```python
import torch

def _softmax(x, axis=-1):
    return torch.softmax(x, dim=axis)
def _gelu(x):
    return torch.nn.functional.gelu(x, approximate="tanh")
def _silu(x):
    return torch.nn.functional.silu(x)
def _sigmoid(x):
    return torch.sigmoid(x)
def _layernorm(x, axis=-1, eps=1e-5):
    m = x.mean(dim=axis, keepdim=True)
    v = x.var(dim=axis, keepdim=True, unbiased=False)
    return (x - m) / torch.sqrt(v + eps)
def _scatter_add(tgt, idx, src, axis=0):
    return tgt.clone().index_add_(axis, idx, src)
def _segment_sum(src, seg, n):
    out = torch.zeros((n,) + src.shape[1:], dtype=src.dtype)
    return out.index_add_(0, seg, src)
def _pad(x, pw):
    flat = tuple(v for pair in reversed(pw) for v in pair)
    return torch.nn.functional.pad(x, flat)

kernel_name = 'gated_mix__linear_kernel_hedgehog__ssm_conv_s4__prompt'

def setup_inputs():

    _w = (lambda a: (lambda t: t.to(torch.bfloat16) if t.is_floating_point() else t)(torch.as_tensor(a)))
    def _A_setup():
        B, S, D, H = 1, 2048, 512, 32
        Dh = D // H
        return {
            "x": _w((lambda *s: torch.randn(*s, dtype=torch.bfloat16) * 0.05)(B, S, D)),
            "wqkv": _w((lambda *s: torch.randn(*s, dtype=torch.bfloat16) * 0.8)(D, 3 * D)),
            "w_phi": _w((lambda *s: torch.randn(*s, dtype=torch.bfloat16) * 0.8)(H, Dh, Dh) * (Dh ** -0.5)),
            "wo": _w((lambda *s: torch.randn(*s, dtype=torch.bfloat16) * 0.05)(D, D)),
        }
    _A_in = _A_setup()

    def _B_setup():
        B, L, D, N = 1, 2048, 512, 128
        A_re = -(lambda _x: torch.exp(torch.as_tensor(_x)))((lambda *s: torch.randn(*s, dtype=torch.bfloat16) * 3.2)(D, N))
        A_im = torch.pi * (lambda *s: torch.randn(*s, dtype=torch.bfloat16) * 0.05)(D, N)
        B_re = (lambda *s: torch.randn(*s, dtype=torch.bfloat16) * 0.2)(D, N)
        B_im = (lambda *s: torch.randn(*s, dtype=torch.bfloat16) * 0.2)(D, N)
        C_re = (lambda *s: torch.randn(*s, dtype=torch.bfloat16) * 0.2)(D, N)
        C_im = (lambda *s: torch.randn(*s, dtype=torch.bfloat16) * 0.2)(D, N)
        log_dt = (lambda *s: torch.randn(*s, dtype=torch.bfloat16) * 0.05)(D) * 0.5 - 4.0
        Dskip = (lambda *s: torch.randn(*s, dtype=torch.bfloat16) * 0.05)(D)
        return {
            "u": _w((lambda *s: torch.randn(*s, dtype=torch.bfloat16) * 0.05)(B, L, D)),
            "A_re": _w(A_re),
            "A_im": _w(A_im),
            "B_re": _w(B_re),
            "B_im": _w(B_im),
            "C_re": _w(C_re),
            "C_im": _w(C_im),
            "log_dt": _w(log_dt),
            "Dskip": _w(Dskip),
        }
    _B_in = _B_setup()

    return {
        "A_x": _A_in["x"],
        "A_wqkv": _A_in["wqkv"],
        "A_w_phi": _A_in["w_phi"],
        "A_wo": _A_in["wo"],
        "B_u": _B_in["u"],
        "B_A_re": _B_in["A_re"],
        "B_A_im": _B_in["A_im"],
        "B_B_re": _B_in["B_re"],
        "B_B_im": _B_in["B_im"],
        "B_C_re": _B_in["C_re"],
        "B_C_im": _B_in["C_im"],
        "B_log_dt": _B_in["log_dt"],
        "B_Dskip": _B_in["Dskip"],
        "gate_w": _w((lambda *s: torch.randn(*s, dtype=torch.bfloat16) * 0.05)(512) * 16.0),
    }

def reference(A_x, A_wqkv, A_w_phi, A_wo, B_u, B_A_re, B_A_im, B_B_re, B_B_im, B_C_re, B_C_im, B_log_dt, B_Dskip, gate_w):
    def _A_ref(x, wqkv, w_phi, wo):
        B, S, D = x.shape
        H = 32; Dh = D // H
        qkv = (lambda x, *d: x.permute(*d))((x @ wqkv).reshape(B, S, 3, H, Dh), 2, 0, 3, 1, 4)
        q, k, v = qkv[0], qkv[1], qkv[2]
        pq = torch.einsum("bhsd,hdf->bhsf", q, w_phi)
        pk = torch.einsum("bhsd,hdf->bhsf", k, w_phi)
        phi_q = torch.concatenate([_softmax(pq, axis=-1), _softmax(-pq, axis=-1)], axis=-1)
        phi_k = torch.concatenate([_softmax(pk, axis=-1), _softmax(-pk, axis=-1)], axis=-1)
        kv = torch.einsum("bhsf,bhsd->bhfd", phi_k, v)
        z  = torch.einsum("bhsf->bhf", phi_k)
        num = torch.einsum("bhqf,bhfd->bhqd", phi_q, kv)
        den = torch.einsum("bhqf,bhf->bhq", phi_q, z)[..., None] + 1e-6
        o = num / den
        return (lambda x, *d: x.permute(*d))(o, 0, 2, 1, 3).reshape(B, S, D) @ wo

    def _B_ref(u, A_re, A_im, B_re, B_im, C_re, C_im, log_dt, Dskip):
        Bsz, L, D = u.shape
        dt = torch.exp(log_dt)[:, None]
        A = A_re + 1j * A_im
        Bc = B_re + 1j * B_im
        Cc = C_re + 1j * C_im
        logdA = dt * A
        dB = dt * Bc
        l = (lambda x, dt: x.to(dt))(torch.arange(L), A_re.dtype)
        P = torch.exp(logdA[:, :, None] * l[None, None, :])
        K = torch.einsum("dn,dnl,dn->dl", Cc, P, dB).real
        L2 = 2 * L
        Kp = _pad(K, ((0, 0), (0, L)))
        up = (lambda x, *d: x.permute(*d))(u, 0, 2, 1)
        up = _pad(up, ((0, 0), (0, 0), (0, L)))
        Kf = (lambda x, axes=(-2,-1): torch.fft.rfft2(x.float(), dim=axes))(Kp[:, None, :], axes=(-2, -1))
        Uf = (lambda x, axes=(-2,-1): torch.fft.rfft2(x.float(), dim=axes))(up[:, :, None, :], axes=(-2, -1))
        Yf = Uf * Kf[None, :, :, :]
        y = (lambda x, s, axes=(-2,-1): torch.fft.irfft2(x, s=s, dim=axes).to(torch.bfloat16))(Yf, (1, L2), axes=(-2, -1))[:, :, 0, :L]
        y = (lambda x, *d: x.permute(*d))(y, 0, 2, 1) + u * Dskip[None, None, :]
        return y

    x = A_x
    g = _sigmoid((x * gate_w).sum(-1, keepdims=True))
    a = _A_ref(x, A_wqkv, A_w_phi, A_wo)
    b = _B_ref(x, B_A_re, B_A_im, B_B_re, B_B_im, B_C_re, B_C_im, B_log_dt, B_Dskip)
    return g * a + (1 - g) * b
```

```cpp
#include <hip/hip_runtime.h>
#include <hip/hip_bf16.h>
typedef unsigned short u16;
__device__ __forceinline__ float b2f(u16 v){ return __uint_as_float(((unsigned)v)<<16); }
__device__ __forceinline__ u16 f2b(float f){ unsigned u=__float_as_uint(f); u += 0x7fffu + ((u>>16)&1u); return (u16)(u>>16); }
__device__ __forceinline__ float rb(float f){ return b2f(f2b(f)); }
constexpr int S=2048,D=512,H=32,N=128;
__global__ void k_qkv(const u16* x,const u16* w,float* qkv){
  int c = blockIdx.x*256+threadIdx.x; int r0 = blockIdx.y*8;
  float acc[8]={0,0,0,0,0,0,0,0};
  for(int k=0;k<D;++k){ float wv=b2f(w[k*1536+c]);
#pragma unroll
    for(int i=0;i<8;++i) acc[i]+=b2f(x[(r0+i)*D+k])*wv; }
#pragma unroll
  for(int i=0;i<8;++i) qkv[(r0+i)*1536+c]=rb(acc[i]);
}
__global__ void k_phi(const float* qkv,const u16* wphi,float* phi){
  int id = blockIdx.x*256+threadIdx.x; int which=id&1; int h=(id>>1)&31; int r=id>>6;
  const float* q = qkv + r*1536 + which*512 + h*16;
  float p[16]; float mx=-1e30f,mn=1e30f;
#pragma unroll
  for(int f=0;f<16;++f){ float a=0;
#pragma unroll
    for(int d=0;d<16;++d) a+=q[d]*b2f(wphi[(h*16+d)*16+f]); p[f]=rb(a); mx=fmaxf(mx,p[f]); mn=fminf(mn,p[f]); }
  float s1=0,s2=0;
#pragma unroll
  for(int f=0;f<16;++f){ s1+=expf(p[f]-mx); s2+=expf(mn-p[f]); }
  float* o = phi + ((size_t)(which*S + r)*32+h)*32;
#pragma unroll
  for(int f=0;f<16;++f){ o[f]=rb(expf(p[f]-mx)/s1); o[16+f]=rb(expf(mn-p[f])/s2); }
}
__global__ void k_kv(const float* qkv,const float* phi,float* kv){
  int h=blockIdx.x; int t=threadIdx.x; if(t>=544) return; int f=t/17,d=t%17; float a=0;
  for(int r=0;r<S;++r){ float pk=phi[((size_t)(S+r)*32+h)*32+f]; a+= pk*(d<16? qkv[r*1536+1024+h*16+d]:1.f); }
  kv[(h*32+f)*17+d]=rb(a);
}
__global__ void k_o(const float* phi,const float* kv,float* o){
  int id=blockIdx.x*256+threadIdx.x; int c=id&511,r=id>>9,h=c>>4,d=c&15; float num=0,den=0;
  for(int f=0;f<32;++f){ float p=phi[((size_t)r*32+h)*32+f]; num+=p*kv[(h*32+f)*17+d]; den+=p*kv[(h*32+f)*17+16]; }
  o[id]=rb(rb(num)/rb(rb(den)+1e-6f));
}
__global__ void k_K(const u16* Are,const u16* Aim,const u16* Bre,const u16* Bim,const u16* Cre,const u16* Cim,const u16* ldt,float* K){
  int d=blockIdx.y; int l=blockIdx.x*256+threadIdx.x; float t=rb((float)l); float dt=rb(expf(b2f(ldt[d]))); float a=0;
  for(int n=0;n<N;++n){ int i=d*N+n; float lr=dt*b2f(Are[i]), li=dt*b2f(Aim[i]); float br=dt*b2f(Bre[i]), bi=dt*b2f(Bim[i]); float cr=b2f(Cre[i]),ci=b2f(Cim[i]);
    float wr=cr*br-ci*bi, wi=cr*bi+ci*br; float e=expf(lr*t); float s,c; sincosf(li*t,&s,&c); a+= e*(wr*c-wi*s); }
  K[d*S+l]=a;
}
__global__ void k_fin(const u16* x,const float* o,const u16* wo,const float* K,const u16* Dsk,const u16* gw,u16* out){
  int r=blockIdx.x; int c=threadIdx.x; __shared__ float red[512];
  red[c]=rb(b2f(x[r*D+c])*b2f(gw[c])); __syncthreads();
  for(int s=256;s>0;s>>=1){ if(c<s) red[c]+=red[c+s]; __syncthreads(); }
  float g=rb(red[0]); g=rb(1.f/(1.f+expf(-g)));
  float a=0; for(int k=0;k<D;++k) a+=o[r*D+k]*b2f(wo[k*D+c]); a=rb(a);
  float y=0; for(int s=0;s<=r;++s) y+=K[c*S+s]*b2f(x[(r-s)*D+c]); y=rb(y);
  float b=rb(y+rb(b2f(x[r*D+c])*b2f(Dsk[c])));
  out[r*D+c]=f2b(rb(g*a)+rb(rb(1.f-g)*b));
}
extern "C" void kernel_launch(void* const* d_in, const int* in_sizes, int n_in, void* d_out, int out_size, void* d_ws, size_t ws_size, hipStream_t stream){
  const u16* x=(const u16*)d_in[0];
  float* ws=(float*)d_ws; float* qkv=ws; float* phi=qkv+S*1536; float* kv=phi+(size_t)2*S*1024; float* o=kv+32*32*17; float* K=o+S*D;
  k_qkv<<<dim3(6,S/8),256,0,stream>>>(x,(const u16*)d_in[1],qkv);
  k_phi<<<S*64/256,256,0,stream>>>(qkv,(const u16*)d_in[2],phi);
  k_kv<<<32,576,0,stream>>>(qkv,phi,kv);
  k_o<<<S*D/256,256,0,stream>>>(phi,kv,o);
  k_K<<<dim3(8,512),256,0,stream>>>((const u16*)d_in[5],(const u16*)d_in[6],(const u16*)d_in[7],(const u16*)d_in[8],(const u16*)d_in[9],(const u16*)d_in[10],(const u16*)d_in[11],K);
  k_fin<<<S,512,0,stream>>>(x,o,(const u16*)d_in[3],K,(const u16*)d_in[12],(const u16*)d_in[13],(u16*)d_out);
}
```

```cpp
#include <hip/hip_runtime.h>
#include <stdint.h>

typedef unsigned short u16;
typedef unsigned int u32;
typedef unsigned long long u64;
typedef short s16x4 __attribute__((ext_vector_type(4)));
typedef __bf16 bf2 __attribute__((ext_vector_type(2)));
typedef __bf16 bf8 __attribute__((ext_vector_type(8)));
typedef float f2 __attribute__((ext_vector_type(2)));
typedef float f4 __attribute__((ext_vector_type(4)));
typedef float f16v __attribute__((ext_vector_type(16)));
typedef u32 v2u __attribute__((ext_vector_type(2)));
typedef u32 v4u __attribute__((ext_vector_type(4)));
#define LAS __attribute__((address_space(3)))
#define GAS __attribute__((address_space(1)))
#define CAS __attribute__((address_space(4)))
#define DI __device__ __forceinline__

#define STAMP(i)
#define STAMP_DECL
#define STAMP_FLUSH(b)

constexpr size_t WS_EPOCH = 0;
constexpr size_t WS_KVP = 8192;
constexpr int KVP_STRIDE = 4608;
constexpr size_t WS_G = WS_KVP + 16 * 16 * KVP_STRIDE;
constexpr size_t WS_XT = 2u << 20;
constexpr size_t WS_O = 6u << 20;
constexpr size_t WS_BT = 8u << 20;
constexpr u32 SPIN_CAP = 1u << 16;

DI u32 pk2(float a, float b) { f2 v = {a, b}; return __builtin_bit_cast(u32, __builtin_convertvector(v, bf2)); }
DI float blo(u32 u) { return __uint_as_float(u << 16); }
DI float bhi(u32 u) { return __uint_as_float(u & 0xffff0000u); }
DI float rbf(float a) { return blo(pk2(a, a)); }
DI f16v mfma32(v4u a, v4u b, f16v c) {
  return __builtin_amdgcn_mfma_f32_32x32x16_bf16(__builtin_bit_cast(bf8, a), __builtin_bit_cast(bf8, b), c, 0, 0, 0);
}
DI f4 mfma16(v4u a, v4u b, f4 c) {
  return __builtin_amdgcn_mfma_f32_16x16x32_bf16(__builtin_bit_cast(bf8, a), __builtin_bit_cast(bf8, b), c, 0, 0, 0);
}
DI v2u trd(LAS char* p) { return __builtin_bit_cast(v2u, __builtin_amdgcn_ds_read_tr16_b64_v4i16((LAS s16x4*)p)); }
DI v4u trd2(LAS char* p0, LAS char* p1) { v2u a = trd(p0), b = trd(p1); return v4u{a.x, a.y, b.x, b.y}; }
DI v4u ld128(LAS char* p) { return *(LAS v4u*)p; }
DI v4u mksrd(const void* p, u32 bytes) { const u64 a = (u64)p; return v4u{(u32)a, (u32)(a >> 32) & 0xffffu, bytes, 0x00020000u}; }
DI void dma16(v4u srd, u32 voff, u32 soff, u32 lds) {
  asm volatile("s_mov_b32 m0, %0\n\ts_nop 0\n\tbuffer_load_dwordx4 %1, %2, %3 offen lds" ::"s"(lds), "v"(voff), "s"(srd), "s"(soff) : "m0", "memory");
}
DI void dma4(v4u srd, u32 voff, u32 soff, u32 lds) {
  asm volatile("s_mov_b32 m0, %0\n\ts_nop 0\n\tbuffer_load_dword %1, %2, %3 offen lds" ::"s"(lds), "v"(voff), "s"(srd), "s"(soff) : "m0", "memory");
}
#define WAITBAR(n) asm volatile("s_waitcnt vmcnt(" #n ") lgkmcnt(0)\n\ts_barrier" ::: "memory")
#define LBAR() asm volatile("s_waitcnt lgkmcnt(0)\n\ts_barrier" ::: "memory")
#define LWAIT() asm volatile("s_waitcnt lgkmcnt(0)" ::: "memory")
DI v4u pack8(const f16v& a, int o) {
  return v4u{pk2(a[o], a[o + 1]), pk2(a[o + 2], a[o + 3]), pk2(a[o + 4], a[o + 5]), pk2(a[o + 6], a[o + 7])};
}
DI __amdgpu_buffer_rsrc_t mkrsrc(const void* p, u32 bytes) {
  return __builtin_amdgcn_make_buffer_rsrc((void*)p, 0, bytes, 0x00020000);
}

DI void dual_softmax(const f16v& acc, int o, v4u& pos, v4u& neg) {
  constexpr float L = 1.4426950408889634f;
  float p[8];
#pragma unroll
  for (int i = 0; i < 4; ++i) { u32 u = pk2(acc[o + 2 * i], acc[o + 2 * i + 1]); p[2 * i] = blo(u); p[2 * i + 1] = bhi(u); }
  float mx = fmaxf(fmaxf(fmaxf(p[0], p[1]), fmaxf(p[2], p[3])), fmaxf(fmaxf(p[4], p[5]), fmaxf(p[6], p[7])));
  float mn = fminf(fminf(fminf(p[0], p[1]), fminf(p[2], p[3])), fminf(fminf(p[4], p[5]), fminf(p[6], p[7])));
  {
    auto r = __builtin_amdgcn_permlane32_swap(__float_as_uint(mx), __float_as_uint(-mn), false, false);
    float m = fmaxf(__uint_as_float(r[0]), __uint_as_float(r[1]));
    auto r2 = __builtin_amdgcn_permlane32_swap(__float_as_uint(m), __float_as_uint(m), false, false);
    mx = __uint_as_float(r2[0]); mn = -__uint_as_float(r2[1]);
  }
  const float cp = -mx * L, cn = mn * L;
  float ep[8], en[8];
#pragma unroll
  for (int i = 0; i < 8; ++i) {
    ep[i] = __builtin_amdgcn_exp2f(__builtin_fmaf(p[i], L, cp));
    en[i] = __builtin_amdgcn_exp2f(__builtin_fmaf(p[i], -L, cn));
  }
  float sp = ((ep[0] + ep[1]) + (ep[2] + ep[3])) + ((ep[4] + ep[5]) + (ep[6] + ep[7]));
  float sn = ((en[0] + en[1]) + (en[2] + en[3])) + ((en[4] + en[5]) + (en[6] + en[7]));
  {
    auto r = __builtin_amdgcn_permlane32_swap(__float_as_uint(sp), __float_as_uint(sn), false, false);
    float m = __builtin_amdgcn_rcpf(__uint_as_float(r[0]) + __uint_as_float(r[1]));
    auto r2 = __builtin_amdgcn_permlane32_swap(__float_as_uint(m), __float_as_uint(m), false, false);
    sp = __uint_as_float(r2[0]); sn = __uint_as_float(r2[1]);
  }
  pos = v4u{pk2(ep[0] * sp, ep[1] * sp), pk2(ep[2] * sp, ep[3] * sp), pk2(ep[4] * sp, ep[5] * sp), pk2(ep[6] * sp, ep[7] * sp)};
  neg = v4u{pk2(en[0] * sn, en[1] * sn), pk2(en[2] * sn, en[3] * sn), pk2(en[4] * sn, en[5] * sn), pk2(en[6] * sn, en[7] * sn)};
}

constexpr int SLOT = 24576;
DI void attn_block(int b, const char* x, const char* wqkv, const char* wphi, char* ws, LAS char* sm) {
  STAMP_DECL; STAMP(0);
  const int tid = threadIdx.x, lane = tid & 63, w = __builtin_amdgcn_readfirstlane(tid >> 6);
  const int xcd = b & 7, j = b >> 3, a = j & 7, c = j >> 3;
  const int rt = (xcd >> 1) * 4 + c, hg = (xcd & 1) * 8 + a;
  const int rot = ((a & 1) + (c & 1) * 2) * 2;
  const u32 tag = *(const CAS u32*)(ws + WS_EPOCH + hg * 256) + 1u;
  const int h = lane >> 5, r32 = lane & 31;
  const int G = lane >> 4, q4 = (lane & 15) >> 2, p4 = lane & 3, blk = G & 1;

  const v4u xd = mksrd(x, 2u << 20), wd = mksrd(wqkv, 1536u * 1024u), pd = mksrd(wphi, 16384u);
  const u32 lb = (u32)(size_t)sm;
  const u32 xb = (rt * 128 + w * 32) * 1024;
  const u32 xo0 = (lane >> 3) * 1024 + (((lane & 7) ^ (lane >> 4)) << 4);
  const u32 xo1 = xo0 ^ 64;
  const u32 wb = hg * 64 + 16 * w * 3072;
  const u32 wo = (lane >> 2) * 3072 + (lane & 3) * 16;
  auto stage = [&](int s0) {
    const int s = (s0 + rot) & 7;
    const u32 base = lb + (s0 % 3) * SLOT;
    const u32 xs = xb + s * 128, wsb = wb + s * (64 * 3072);
#pragma unroll
    for (int i = 0; i < 4; ++i) dma16(xd, (i & 1) ? xo1 : xo0, xs + i * 8192, base + w * 4096 + i * 1024);
    dma16(wd, wo, wsb + 1024, base + 16384 + w * 1024);
    dma16(wd, wo, wsb + 2048, base + 20480 + w * 1024);
  };
  auto stage_q = [&](int s0) {
    const int s = (s0 + rot) & 7;
    dma16(wd, wo, wb + s * (64 * 3072), lb + 2 * SLOT + s0 * 4096 + w * 1024);
  };
  dma4(pd, tid * 4, hg * 1024, lb + 3 * SLOT + w * 256);
  stage(0); stage(1); stage(2);

  LAS char* xr[4];
#pragma unroll
  for (int ks = 0; ks < 4; ++ks) xr[ks] = sm + w * 4096 + r32 * 128 + (((2 * ks + h) ^ ((r32 >> 1) & 7)) << 4);
  LAS char* wr = sm + (8 * h + q4) * 64 + blk * 32 + p4 * 8;

  v4u xf[32];
  v4u wph;
  f16v ak = {}, av = {};
#pragma unroll
  for (int s0 = 0; s0 < 8; ++s0) {
    if (s0 == 0) WAITBAR(12);
    else if (s0 == 2 || s0 == 3) WAITBAR(8);
    else WAITBAR(6);
    if (s0 == 0) STAMP(1);
    if (s0 >= 1 && s0 <= 5) stage(s0 + 2);
    if (s0 == 1) { stage_q(6); stage_q(7); }
    if (s0 == 6) {
#pragma unroll
      for (int i = 0; i < 6; ++i) stage_q(i);
    }
    if (s0 == 0) {
      LAS char* pp = sm + 3 * SLOT + blk * 512 + (4 * h + q4) * 32 + p4 * 8;
      wph = trd2(pp, pp + 256);
    }
    const int so = (s0 % 3) * SLOT;
#pragma unroll
    for (int ks = 0; ks < 4; ++ks) {
      xf[s0 * 4 + ks] = ld128(xr[ks] + so);
      v4u fk = trd2(wr + so + 16384 + ks * 1024, wr + so + 16384 + ks * 1024 + 256);
      v4u fv = trd2(wr + so + 20480 + ks * 1024, wr + so + 20480 + ks * 1024 + 256);
      ak = mfma32(fk, xf[s0 * 4 + ks], ak);
      av = mfma32(xf[s0 * 4 + ks], fv, av);
    }
  }
  STAMP(2);
  const v4u zero4 = {0, 0, 0, 0};
  const v4u wp0 = blk == 0 ? wph : zero4, wp1 = blk == 0 ? zero4 : wph;
  {
    f16v pk = {};
    pk = mfma32(wp0, pack8(ak, 0), pk);
    pk = mfma32(wp1, pack8(ak, 8), pk);
    LAS char* img = sm + SLOT + w * 4096;
    const int sw = (r32 >> 1) & 7;
#pragma unroll
    for (int hd = 0; hd < 2; ++hd) {
      v4u pos, neg;
      dual_softmax(pk, 8 * hd, pos, neg);
      LAS char* row = img + hd * 2048 + r32 * 64;
      *(LAS v2u*)(row + ((h ^ sw) << 3)) = v2u{pos.x, pos.y};
      *(LAS v2u*)(row + (((2 + h) ^ sw) << 3)) = v2u{pos.z, pos.w};
      *(LAS v2u*)(row + (((4 + h) ^ sw) << 3)) = v2u{neg.x, neg.y};
      *(LAS v2u*)(row + (((6 + h) ^ sw) << 3)) = v2u{neg.z, neg.w};
    }
    LWAIT();
    const v4u ones = {0x3f803f80u, 0x3f803f80u, 0x3f803f80u, 0x3f803f80u};
    const v4u vf0 = pack8(av, 0), vf1 = pack8(av, 8);
    LAS char* scr = sm + w * 5120 + r32 * 80 + h * 16;
#pragma unroll
    for (int hd = 0; hd < 2; ++hd) {
      f16v kv = {};
#pragma unroll
      for (int s = 0; s < 2; ++s) {
        const int t0 = 16 * s + 4 * h + q4, t1 = t0 + 8;
        v4u bfr = trd2(img + hd * 2048 + t0 * 64 + (((4 * blk + p4) ^ ((t0 >> 1) & 7)) << 3),
                       img + hd * 2048 + t1 * 64 + (((4 * blk + p4) ^ ((t1 >> 1) & 7)) << 3));
        v4u afr = (blk == hd) ? (s ? vf1 : vf0) : ones;
        kv = mfma32(afr, bfr, kv);
      }
      const int o = 8 * hd;
      *(LAS f4*)(scr + hd * 2560) = f4{kv[o], kv[o + 1], kv[o + 2], kv[o + 3]};
      *(LAS f4*)(scr + hd * 2560 + 32) = f4{kv[o + 4], kv[o + 5], kv[o + 6], kv[o + 7]};
      if (h == 0) *(LAS float*)(scr + hd * 2560 + 64) = kv[8 - o];
    }
  }
  WAITBAR(0);
  const __amdgpu_buffer_rsrc_t kvr = mkrsrc(ws + WS_KVP + (size_t)hg * 16 * KVP_STRIDE, 16 * KVP_STRIDE);
  {
    LAS char* p = sm + (tid >> 2) * 80 + (tid & 3) * 16;
    f4 s = (*(LAS f4*)p + *(LAS f4*)(p + 5120)) + (*(LAS f4*)(p + 10240) + *(LAS f4*)(p + 15360));
    __builtin_amdgcn_raw_buffer_store_b128(v4u{pk2(s[0], s[1]), tag, pk2(s[2], s[3]), tag}, kvr, rt * KVP_STRIDE + tid * 16, 0, 16);
    if (tid < 32) {
      LAS char* z = sm + tid * 160 + 64;
      float z0 = (*(LAS float*)z + *(LAS float*)(z + 5120)) + (*(LAS float*)(z + 10240) + *(LAS float*)(z + 15360));
      z += 80;
      float z1 = (*(LAS float*)z + *(LAS float*)(z + 5120)) + (*(LAS float*)(z + 10240) + *(LAS float*)(z + 15360));
      __builtin_amdgcn_raw_buffer_store_b128(v4u{__float_as_uint(z0), tag, __float_as_uint(z1), tag}, kvr,
                                             rt * KVP_STRIDE + 4096 + tid * 16, 0, 16);
    }
  }
  STAMP(3);
  f16v aq = {};
#pragma unroll
  for (int s0 = 0; s0 < 8; ++s0)
#pragma unroll
    for (int ks = 0; ks < 4; ++ks) {
      v4u fq = trd2(wr + 2 * SLOT + s0 * 4096 + ks * 1024, wr + 2 * SLOT + s0 * 4096 + ks * 1024 + 256);
      aq = mfma32(fq, xf[s0 * 4 + ks], aq);
    }
  STAMP(4);
  v4u rec[16], zr[4];
  const u32 ro = tid * 16, zo = (tid & 3) * 4 * KVP_STRIDE + 4096 + (tid >> 3) * 16;
  auto sweep = [&]() {
#pragma unroll
    for (int p = 0; p < 16; ++p) rec[p] = __builtin_amdgcn_raw_buffer_load_b128(kvr, ro, p * KVP_STRIDE, 16);
#pragma unroll
    for (int p = 0; p < 4; ++p) zr[p] = __builtin_amdgcn_raw_buffer_load_b128(kvr, zo, p * KVP_STRIDE, 16);
  };
  __builtin_amdgcn_sched_barrier(0);
  sweep();
  __builtin_amdgcn_sched_barrier(0);
  v4u qp[2], qn[2];
  {
    f16v pq = {};
    pq = mfma32(wp0, pack8(aq, 0), pq);
    pq = mfma32(wp1, pack8(aq, 8), pq);
    dual_softmax(pq, 0, qp[0], qn[0]);
    dual_softmax(pq, 8, qp[1], qn[1]);
  }
  STAMP(5);
  for (u32 spins = 0;; ++spins) {
    u32 bad = 0;
#pragma unroll
    for (int p = 0; p < 16; ++p) bad |= (rec[p].y ^ tag) | (rec[p].w ^ tag);
#pragma unroll
    for (int p = 0; p < 4; ++p) bad |= (zr[p].y ^ tag) | (zr[p].w ^ tag);
    if (!__any(bad != 0) || spins >= SPIN_CAP) break;
    __builtin_amdgcn_s_sleep(2);
    asm volatile("" ::: "memory");
    sweep();
  }
  STAMP(6);
  {
    float s0 = 0, s1 = 0, s2 = 0, s3 = 0, z = 0;
#pragma unroll
    for (int p = 0; p < 16; ++p) { s0 += blo(rec[p].x); s1 += bhi(rec[p].x); s2 += blo(rec[p].z); s3 += bhi(rec[p].z); }
#pragma unroll
    for (int p = 0; p < 4; ++p) z += __uint_as_float((tid & 4) ? zr[p].z : zr[p].x);
    z += __uint_as_float(__builtin_amdgcn_mov_dpp(__float_as_uint(z), 0xB1, 0xf, 0xf, true));
    z += __uint_as_float(__builtin_amdgcn_mov_dpp(__float_as_uint(z), 0x4E, 0xf, 0xf, true));
    LAS char* p = sm + SLOT + 16384 + (tid >> 2) * 64 + (tid & 3) * 8;
    const u32 zz = pk2(z, z);
    *(LAS v2u*)p = v2u{pk2(s0, s1), pk2(s2, s3)};
    *(LAS v2u*)(p + 32) = v2u{zz, zz};
  }
  LBAR();
  if (rt == 0 && tid == 0) __hip_atomic_store((GAS u32*)(ws + WS_EPOCH + hg * 256), tag, __ATOMIC_RELAXED, __HIP_MEMORY_SCOPE_AGENT);
  {
    LAS char* im = sm + SLOT + 16384 + (4 * h + q4) * 64 + blk * 32 + p4 * 8;
    char* ob = ws + WS_O + ((size_t)(rt * 4 + w) * 32 + hg * 2) * 1024 + lane * 16;
#pragma unroll
    for (int hd = 0; hd < 2; ++hd) {
      f16v nm = {};
      nm = mfma32(trd2(im + hd * 2048, im + hd * 2048 + 512), qp[hd], nm);
      nm = mfma32(trd2(im + hd * 2048 + 1024, im + hd * 2048 + 1536), qn[hd], nm);
      const float inv = __builtin_amdgcn_rcpf(rbf(rbf(nm[8]) + 1e-6f));
      v4u nb = pack8(nm, 0);
      v4u o = {pk2(blo(nb.x) * inv, bhi(nb.x) * inv), pk2(blo(nb.y) * inv, bhi(nb.y) * inv),
               pk2(blo(nb.z) * inv, bhi(nb.z) * inv), pk2(blo(nb.w) * inv, bhi(nb.w) * inv)};
      *(GAS v4u*)(ob + hd * 1024) = o;
    }
  }
  STAMP(7);
  STAMP_FLUSH(b);
}

DI f2 cmul(f2 a, f2 b) { return f2{a.x, a.x} * b + f2{-a.y, a.y} * f2{b.y, b.x}; }
DI f2 cexp2(float lr, float li, float t) {
  const float e = __builtin_amdgcn_exp2f(lr * t), ph = __builtin_amdgcn_fractf(li * t);
  return f2{e, e} * f2{__builtin_amdgcn_cosf(ph), __builtin_amdgcn_sinf(ph)};
}
DI u32 kc_index(int lag) {
  const int v = (int)rbf((float)lag);
  const int col = v & 0xf0, lo = (v >> 2) & 3;
  const int i1 = 256 + col + ((v & 2) ? 12 : 0) + lo;
  const int i2 = 256 + col + 4 + ((v >> 6) & 4) + lo;
  const int i3 = 512 + col + ((v & 8) ? 12 : 0) + ((v >> 8) & 3);
  int r = v < 256 ? v : v < 512 ? i1 : v < 1024 ? i2 : v < 2048 ? i3 : 516;
  return lag < 0 ? 768 : r;
}
constexpr int S_TBL = 0, TBL_CH = 8448, TBL_ODD = 4160;
constexpr int S_RED = 32768;
constexpr int S_U = 65536, U_CH = 4608;
constexpr int S_KC = 75776, KC_CH = 2048;

DI void ssm_block(int s, const char* x, const char* Are, const char* Aim, const char* Bre, const char* Bim,
                  const char* Cre, const char* Cim, const char* logdt, const char* dsk, const char* gw, char* ws,
                  LAS char* sm) {
  STAMP_DECL; STAMP(0);
  const int tid = threadIdx.x, lane = tid & 63, w = __builtin_amdgcn_readfirstlane(tid >> 6);
  const int grp = s >> 5, ta = s & 31;
  const u32 tag = *(const CAS u32*)(ws + WS_EPOCH + (16 + grp) * 256) + 1u;
  const int G = lane >> 4, q4 = (lane & 15) >> 2, p4 = lane & 3;
  const int ch = w >> 1, type = (w & 1) * 2 + (lane >> 5), q = lane & 31;
  const char* xt = x + (size_t)(ta * 64 + (tid >> 3)) * 1024 + grp * 128 + (tid & 7) * 16;
  const v4u t0 = *(const GAS v4u*)xt, t1 = *(const GAS v4u*)(xt + 32768);
  const u32 po = (2 * s + ch) * 256 + q * 8;
  const v2u are = *(const GAS v2u*)(Are + po), aim = *(const GAS v2u*)(Aim + po);
  const v2u bre = *(const GAS v2u*)(Bre + po), bim = *(const GAS v2u*)(Bim + po);
  const v2u cre = *(const GAS v2u*)(Cre + po), cim = *(const GAS v2u*)(Cim + po);
  const u32 ldt2 = *(const CAS u32*)(logdt + s * 4), dsk2 = *(const CAS u32*)(dsk + s * 4);
  const char* gx = x + (size_t)(s * 8 + w * 2) * 1024 + lane * 16;
  const v4u g0 = *(const GAS v4u*)gx, g1 = *(const GAS v4u*)(gx + 1024), gwv = *(const GAS v4u*)(gw + lane * 16);
  u32 ki[9];
#pragma unroll
  for (int e = 0; e < 9; ++e) ki[e] = kc_index(2047 - 8 * tid - e) * 2;
  {
    const int r0 = tid >> 3, r1 = r0 + 32, cc = tid & 7;
    *(LAS v4u*)(sm + S_U + r0 * 128 + ((cc ^ (2 * ((r0 >> 1) & 3))) << 4)) = t0;
    *(LAS v4u*)(sm + S_U + r1 * 128 + ((cc ^ (2 * ((r1 >> 1) & 3))) << 4)) = t1;
    if (tid < 2) *(LAS u32*)(sm + S_KC + tid * KC_CH + 1536) = 0;
    LBAR();
    STAMP(1);
    LAS char* im2 = sm + w * 2048;
    const int ci = lane & 15;
#pragma unroll
    for (int k = 0; k < 4; ++k) {
      const int t = 16 * G + 4 * k + q4;
      v2u v = trd(sm + S_U + t * 128 + (((2 * w + (p4 >> 1)) ^ (2 * ((t >> 1) & 3))) << 4) + (p4 & 1) * 8);
      *(LAS v2u*)(im2 + ci * 128 + (((4 * G + k) ^ ci) << 3)) = v;
    }
    LWAIT();
    const __amdgpu_buffer_rsrc_t xr = mkrsrc(ws + WS_XT, 4u << 20);
#pragma unroll
    for (int k = 0; k < 4; ++k) {
      const int c = 4 * k + G;
      v2u v = *(LAS v2u*)(im2 + c * 128 + ((ci ^ c) << 3));
      __builtin_amdgcn_raw_buffer_store_b128(v4u{v.x, tag, v.y, tag}, xr, (grp * 64 + w * 16 + c) * 8192 + ta * 256 + ci * 16, 0, 16);
    }
  }
  {
    float a0 = 0, a1 = 0;
#pragma unroll
    for (int i = 0; i < 4; ++i) {
      u32 u0 = pk2(blo(g0[i]) * blo(gwv[i]), bhi(g0[i]) * bhi(gwv[i]));
      u32 u1 = pk2(blo(g1[i]) * blo(gwv[i]), bhi(g1[i]) * bhi(gwv[i]));
      a0 += blo(u0) + bhi(u0); a1 += blo(u1) + bhi(u1);
    }
    auto r = __builtin_amdgcn_permlane32_swap(__float_as_uint(a0), __float_as_uint(a1), false, false);
    float v = __uint_as_float(r[0]) + __uint_as_float(r[1]);
#pragma unroll
    for (int m = 16; m >= 1; m >>= 1) v += __shfl_xor(v, m);
    v = rbf(v);
    v = rbf(__builtin_amdgcn_rcpf(1.f + __builtin_amdgcn_exp2f(-1.4426950408889634f * v)));
    if ((lane & 31) == 0) *(GAS float*)(ws + WS_G + (s * 8 + w * 2 + (lane >> 5)) * 4) = v;
  }
  STAMP(2);
  {
    const float dt = rbf(expf(ch ? bhi(ldt2) : blo(ldt2)));
    const bool isA = type == 0;
    const float tG = type < 2 ? 0.f : type == 2 ? 256.f : 1024.f;
    const float tP = type == 0 ? 192.f : type == 1 ? 12.f : type == 2 ? 2.f : 8.f;
    const float tQ = type == 0 ? 64.f : type == 1 ? 4.f : type == 2 ? 256.f : 1024.f;
    const float tR = type == 0 ? 16.f : type == 1 ? 1.f : type == 2 ? 4.f : 256.f;
    const float si = isA ? -0.15915494309189535f : 0.15915494309189535f;
    u32 ev[16][4];
#pragma unroll
    for (int n = 0; n < 4; ++n) {
      auto el = [&](const v2u& v) { return (n & 1) ? bhi(v[n >> 1]) : blo(v[n >> 1]); };
      const float lr = (dt * el(are)) * 1.4426950408889634f, li = (dt * el(aim)) * si;
      const float br = dt * el(bre), bi = dt * el(bim), cr = el(cre), ci = el(cim);
      const f2 W = {isA ? cr * br - ci * bi : 1.f, isA ? -(cr * bi + ci * br) : 0.f};
      f2 g[4], r[4];
      g[0] = cmul(cexp2(lr, li, tG), W);
      const f2 P = cexp2(lr, li, tP), Q = cexp2(lr, li, tQ);
      r[1] = cexp2(lr, li, tR);
      g[3] = cmul(g[0], P); g[1] = cmul(g[0], Q); g[2] = cmul(g[1], Q);
      r[2] = cmul(r[1], r[1]); r[3] = cmul(r[2], r[1]);
#pragma unroll
      for (int gi = 0; gi < 4; ++gi) {
        ev[4 * gi][n] = pk2(g[gi].x, g[gi].y);
#pragma unroll
        for (int ri = 1; ri < 4; ++ri) { const f2 v = cmul(g[gi], r[ri]); ev[4 * gi + ri][n] = pk2(v.x, v.y); }
      }
    }
    LBAR();
    LAS char* eb = sm + (ch * 4 + type) * 8192 + q * 256;
#pragma unroll
    for (int sl = 0; sl < 16; ++sl) *(LAS v4u*)(eb + ((sl ^ (q & 15)) << 4)) = v4u{ev[sl][0], ev[sl][1], ev[sl][2], ev[sl][3]};
  }
  LBAR();
  STAMP(3);
  const __amdgpu_buffer_rsrc_t ur = mkrsrc(ws + WS_XT + (size_t)s * 16384, 16384);
  v4u ur4[4];
  auto sweep = [&]() {
#pragma unroll
    for (int k = 0; k < 4; ++k) ur4[k] = __builtin_amdgcn_raw_buffer_load_b128(ur, tid * 16, k * 4096, 16);
  };
  sweep();
  {
    const int c2 = w & 1, r = lane & 15;
    LAS char* eb = sm + c2 * 32768 + G * 256;
    const int tl = w < 2 ? 0 : 1;
    f4 k0 = {}, k1 = {};
#pragma unroll
    for (int ks = 0; ks < 8; ++ks) {
      const int off = ks * 1024 + ((r ^ ((4 * ks + G) & 15)) << 4);
      const v4u fa = ld128(eb + off);
      k0 = mfma16(ld128(eb + (1 + tl) * 8192 + off), fa, k0);
      if (w < 2) k1 = mfma16(ld128(eb + 3 * 8192 + off), fa, k1);
    }
    LAS char* kc = sm + S_KC + c2 * KC_CH + r * 32 + G * 8;
    *(LAS v2u*)(kc + tl * 512) = v2u{pk2(k0[0], k0[1]), pk2(k0[2], k0[3])};
    if (w < 2) *(LAS v2u*)(kc + 1024) = v2u{pk2(k1[0], k1[1]), pk2(k1[2], k1[3])};
  }
  LBAR();
  STAMP(4);
#pragma unroll
  for (int c2 = 0; c2 < 2; ++c2) {
    u32 kv[9];
#pragma unroll
    for (int e = 0; e < 9; ++e) kv[e] = *(LAS u16*)(sm + S_KC + c2 * KC_CH + ki[e]);
    LAS char* tb = sm + S_TBL + c2 * TBL_CH + tid * 16;
    *(LAS v4u*)tb = v4u{kv[0] | (kv[1] << 16), kv[2] | (kv[3] << 16), kv[4] | (kv[5] << 16), kv[6] | (kv[7] << 16)};
    *(LAS v4u*)(tb + TBL_ODD) = v4u{kv[1] | (kv[2] << 16), kv[3] | (kv[4] << 16), kv[5] | (kv[6] << 16), kv[7] | (kv[8] << 16)};
  }
  if (tid < 8) *(LAS v4u*)(sm + S_TBL + (tid >> 2) * TBL_CH + ((tid >> 1) & 1) * TBL_ODD + 4096 + (tid & 1) * 16) = v4u{0, 0, 0, 0};
  if (tid >= 64 && tid < 124) {
    const int i = tid - 64;
    *(LAS v4u*)(sm + S_U + (i >= 30 ? U_CH + (i - 30) * 16 : i * 16)) = v4u{0, 0, 0, 0};
  }
  for (u32 spins = 0;; ++spins) {
    u32 bad = 0;
#pragma unroll
    for (int k = 0; k < 4; ++k) bad |= (ur4[k].y ^ tag) | (ur4[k].w ^ tag);
    if (!__any(bad != 0) || spins >= SPIN_CAP) break;
    __builtin_amdgcn_s_sleep(2);
    asm volatile("" ::: "memory");
    sweep();
  }
#pragma unroll
  for (int k = 0; k < 4; ++k)
    *(LAS v2u*)(sm + S_U + (k >> 1) * U_CH + 480 + ((k & 1) * 256 + tid) * 8) = v2u{ur4[k].x, ur4[k].z};
  LBAR();
  STAMP(5);
  if (ta == 0 && tid == 0) __hip_atomic_store((GAS u32*)(ws + WS_EPOCH + (16 + grp) * 256), tag, __ATOMIC_RELAXED, __HIP_MEMORY_SCOPE_AGENT);
  const int rh = w & 1, m = lane & 15;
  f4 acc[8] = {};
  {
    LAS char* ub = sm + S_U + ch * U_CH + m * 32 + G * 16 + (3 - 4 * rh) * 64 + 64;
    const int s0 = 2031 + 8 * G - m;
    LAS char* kb = sm + S_TBL + ch * TBL_CH + ((s0 & 1) ? TBL_ODD : 0) + (s0 >> 1) * 4 - rh * 256 - 59 * 64;
#pragma unroll
    for (int rr = 0; rr < 4; ++rr) {
      v4u bf[8];
#pragma unroll
      for (int i = 0; i < 8; ++i) bf[i] = ld128(ub + i * 512 + (3 - rr) * 64);
#pragma unroll
      for (int J = 0; J < 8; ++J) {
        LAS u32* kp = (LAS u32*)(kb + (59 - 8 * J - rr) * 64);
        const v4u af = {kp[0], kp[1], kp[2], kp[3]};
#pragma unroll
        for (int i = J; i < 8; ++i) acc[i] = mfma16(af, bf[i - J], acc[i]);
      }
    }
  }
  STAMP(6);
  {
    LAS char* mine = sm + S_RED + w * 4096 + lane * 16;
    LAS char* other = sm + S_RED + (w ^ 1) * 4096 + lane * 16;
#pragma unroll
    for (int i = 0; i < 4; ++i) {
      f4 give;
#pragma unroll
      for (int e = 0; e < 4; ++e) { const float lo = acc[i][e], hi = acc[4 + i][e]; give[e] = rh ? lo : hi; acc[i][e] = rh ? hi : lo; }
      *(LAS f4*)(mine + i * 1024) = give;
    }
    LBAR();
    const float dk = ch ? bhi(dsk2) : blo(dsk2);
    LAS char* up = sm + S_U + ch * U_CH + 480 + rh * 2048 + m * 32 + G * 8;
    char* bo = ws + WS_BT + (size_t)(2 * s + ch) * 4096 + rh * 2048 + m * 32 + G * 8;
#pragma unroll
    for (int i = 0; i < 4; ++i) {
      const f4 y = acc[i] + *(LAS f4*)(other + i * 1024);
      const v2u u = *(LAS v2u*)(up + i * 512);
      const u32 y0 = pk2(y[0], y[1]), y1 = pk2(y[2], y[3]);
      const u32 d0 = pk2(blo(u.x) * dk, bhi(u.x) * dk), d1 = pk2(blo(u.y) * dk, bhi(u.y) * dk);
      *(GAS v2u*)(bo + i * 512) = v2u{pk2(blo(y0) + blo(d0), bhi(y0) + bhi(d0)), pk2(blo(y1) + blo(d1), bhi(y1) + bhi(d1))};
    }
  }
  STAMP(7);
  STAMP_FLUSH(256 + s);
}

__global__ __launch_bounds__(256, 2) void k_front(const char* x, const char* wqkv, const char* wphi, const char* Are,
                                                   const char* Aim, const char* Bre, const char* Bim, const char* Cre,
                                                   const char* Cim, const char* logdt, const char* dsk, const char* gw,
                                                   char* ws) {
  extern __shared__ __attribute__((aligned(16))) char smem[];
  LAS char* sm = (LAS char*)smem;
  if (blockIdx.x < 256) attn_block(blockIdx.x, x, wqkv, wphi, ws, sm);
  else ssm_block(blockIdx.x - 256, x, Are, Aim, Bre, Bim, Cre, Cim, logdt, dsk, gw, ws, sm);
}

constexpr int B_W = 65536, B_B = 131072, B_G = 139264, B_LDS = 140288;
__global__ __launch_bounds__(256, 1) void k_back(const char* wo, const char* ws, char* out) {
  extern __shared__ __attribute__((aligned(16))) char smem[];
  LAS char* sm = (LAS char*)smem;
  STAMP_DECL; STAMP(0);
  const int tid = threadIdx.x, lane = tid & 63, w = __builtin_amdgcn_readfirstlane(tid >> 6);
  const int b = blockIdx.x, xcd = b & 7, j = b >> 3;
  const int tb = (xcd >> 1) * 8 + (j >> 2), cb = (xcd & 1) * 4 + (j & 3);
  const int rot = (j + (j >> 2)) & 3;
  const int wc = w & 1, wt = w >> 1;
  const int h = lane >> 5, G = lane >> 4, q4 = (lane & 15) >> 2, p4 = lane & 3, blk = G & 1;
  const u32 lo = lane * 16;
  const u32 so = (lane >> 3) * 1024 + (((lane & 7) ^ (((lane >> 4) & 1) << 2)) << 4);
  const v4u sd = mksrd(ws, 10u << 20), wd = mksrd(wo, 512u * 1024u);
  const u32 lb = (u32)(size_t)sm;
  const u32 ob = (u32)WS_O + ((tb * 2 + (w >> 1)) * 32 + (w & 1) * 4) * 1024;
  const u32 wb = cb * 128 + w * 32 * 1024;
#pragma unroll
  for (int s0 = 0; s0 < 4; ++s0) {
    const int s = (s0 + rot) & 3;
#pragma unroll
    for (int i = 0; i < 4; ++i) dma16(sd, lo, ob + (s * 8 + i) * 1024, lb + s0 * 16384 + w * 4096 + i * 1024);
#pragma unroll
    for (int i = 0; i < 4; ++i) dma16(wd, so, wb + s * 131072 + i * 8192, lb + B_W + s0 * 16384 + w * 4096 + i * 1024);
  }
  {
    const u32 bo = (lane >> 3) * 4096 + (((lane & 7) ^ (((lane >> 4) & 1) << 2)) << 4);
    const u32 bb = (u32)WS_BT + (cb * 64 + w * 16) * 4096 + tb * 128;
    dma16(sd, bo, bb, lb + B_B + w * 2048);
    dma16(sd, bo, bb + 8 * 4096, lb + B_B + w * 2048 + 1024);
    dma4(sd, lane * 4, (u32)WS_G + tb * 256, lb + B_G + w * 256);
  }
  const int sx = (4 * wc + 2 * blk + (p4 >> 1)) ^ ((q4 >> 1) << 2);
  LAS char* wr = sm + B_W + (4 * h + q4) * 128 + sx * 16 + (p4 & 1) * 8;
  LAS char* orr = sm + wt * 8192 + lo;
  f16v acc = {};
#pragma unroll
  for (int s0 = 0; s0 < 4; ++s0) {
    if (s0 == 0) WAITBAR(27);
    else if (s0 == 1) WAITBAR(19);
    else if (s0 == 2) WAITBAR(11);
    else WAITBAR(0);
    if (s0 == 0) STAMP(1);
#pragma unroll
    for (int hh = 0; hh < 8; ++hh)
      acc = mfma32(trd2(wr + s0 * 16384 + hh * 2048, wr + s0 * 16384 + hh * 2048 + 1024), ld128(orr + s0 * 16384 + hh * 1024), acc);
  }
  STAMP(2);
  const int t = wt * 32 + (lane & 31);
  const float g = *(LAS float*)(sm + B_G + t * 4), g1 = rbf(1.f - g);
  const int bx = (4 * wt + 2 * blk + (p4 >> 1)) ^ ((q4 >> 1) << 2);
  LAS char* br = sm + B_B + (32 * wc + 4 * h + q4) * 128 + bx * 16 + (p4 & 1) * 8;
  v2u o[4];
#pragma unroll
  for (int k = 0; k < 4; ++k) {
    const v2u bv = trd(br + k * 1024);
    const u32 a0 = pk2(acc[4 * k], acc[4 * k + 1]), a1 = pk2(acc[4 * k + 2], acc[4 * k + 3]);
    const u32 m0 = pk2(g * blo(a0), g * bhi(a0)), m1 = pk2(g * blo(a1), g * bhi(a1));
    const u32 n0 = pk2(g1 * blo(bv.x), g1 * bhi(bv.x)), n1 = pk2(g1 * blo(bv.y), g1 * bhi(bv.y));
    o[k] = v2u{pk2(blo(m0) + blo(n0), bhi(m0) + bhi(n0)), pk2(blo(m1) + blo(n1), bhi(m1) + bhi(n1))};
  }
  char* op = out + (size_t)(tb * 64 + t) * 1024 + cb * 128 + wc * 64 + h * 16;
#pragma unroll
  for (int k = 0; k < 4; k += 2) {
    auto rx = __builtin_amdgcn_permlane32_swap(o[k].x, o[k + 1].x, false, false);
    auto ry = __builtin_amdgcn_permlane32_swap(o[k].y, o[k + 1].y, false, false);
    *(GAS v4u*)(op + k * 16) = v4u{rx[0], ry[0], rx[1], ry[1]};
  }
  STAMP(3);
  STAMP_FLUSH(512 + b);
}

extern "C" void kernel_launch(void* const* d_in, const int* in_sizes, int n_in, void* d_out, int out_size, void* d_ws,
                              size_t ws_size, hipStream_t stream) {
  (void)in_sizes; (void)n_in; (void)out_size; (void)ws_size;
  (void)hipFuncSetAttribute((const void*)k_front, hipFuncAttributeMaxDynamicSharedMemorySize, 81920);
  (void)hipFuncSetAttribute((const void*)k_back, hipFuncAttributeMaxDynamicSharedMemorySize, B_LDS);
  const char* const* in = (const char* const*)d_in;
  k_front<<<512, 256, 81920, stream>>>(in[0], in[1], in[2], in[5], in[6], in[7], in[8], in[9], in[10], in[11], in[12],
                                       in[13], (char*)d_ws);
  k_back<<<256, 256, B_LDS, stream>>>(in[3], (const char*)d_ws, (char*)d_out);
}
```

```cpp
#include <hip/hip_runtime.h>
#include <stdint.h>

typedef unsigned short u16;
typedef unsigned int u32;
typedef unsigned long long u64;
typedef short s16x4 __attribute__((ext_vector_type(4)));
typedef __bf16 bf2 __attribute__((ext_vector_type(2)));
typedef __bf16 bf8 __attribute__((ext_vector_type(8)));
typedef float f2 __attribute__((ext_vector_type(2)));
typedef float f4 __attribute__((ext_vector_type(4)));
typedef float f16v __attribute__((ext_vector_type(16)));
typedef u32 v2u __attribute__((ext_vector_type(2)));
typedef u32 v4u __attribute__((ext_vector_type(4)));
#define LAS __attribute__((address_space(3)))
#define GAS __attribute__((address_space(1)))
#define CAS __attribute__((address_space(4)))
#define DI __device__ __forceinline__

#define STAMP(i)
#define STAMP_DECL
#define STAMP_FLUSH(b)

constexpr size_t WS_EPOCH = 0;
constexpr size_t WS_KVP = 8192;
constexpr int KVP_STRIDE = 4608;
constexpr size_t WS_G = WS_KVP + 16 * 16 * KVP_STRIDE;
constexpr size_t WS_XT = 2u << 20;
constexpr size_t WS_O = 6u << 20;
constexpr size_t WS_BT = 8u << 20;
constexpr u32 SPIN_CAP = 1u << 16;

DI u32 pk2(float a, float b) { f2 v = {a, b}; return __builtin_bit_cast(u32, __builtin_convertvector(v, bf2)); }
DI float blo(u32 u) { return __uint_as_float(u << 16); }
DI float bhi(u32 u) { return __uint_as_float(u & 0xffff0000u); }
DI float rbf(float a) { return blo(pk2(a, a)); }
DI f16v mfma32(v4u a, v4u b, f16v c) {
  return __builtin_amdgcn_mfma_f32_32x32x16_bf16(__builtin_bit_cast(bf8, a), __builtin_bit_cast(bf8, b), c, 0, 0, 0);
}
DI f4 mfma16(v4u a, v4u b, f4 c) {
  return __builtin_amdgcn_mfma_f32_16x16x32_bf16(__builtin_bit_cast(bf8, a), __builtin_bit_cast(bf8, b), c, 0, 0, 0);
}
DI v2u trd(LAS char* p) { return __builtin_bit_cast(v2u, __builtin_amdgcn_ds_read_tr16_b64_v4i16((LAS s16x4*)p)); }
DI v4u trd2(LAS char* p0, LAS char* p1) { v2u a = trd(p0), b = trd(p1); return v4u{a.x, a.y, b.x, b.y}; }
DI v4u ld128(LAS char* p) { return *(LAS v4u*)p; }
DI v4u mksrd(const void* p, u32 bytes) { const u64 a = (u64)p; return v4u{(u32)a, (u32)(a >> 32) & 0xffffu, bytes, 0x00020000u}; }
DI void dma16(v4u srd, u32 voff, u32 soff, u32 lds) {
  asm volatile("s_mov_b32 m0, %0\n\ts_nop 0\n\tbuffer_load_dwordx4 %1, %2, %3 offen lds" ::"s"(lds), "v"(voff), "s"(srd), "s"(soff) : "m0", "memory");
}
DI void dma4(v4u srd, u32 voff, u32 soff, u32 lds) {
  asm volatile("s_mov_b32 m0, %0\n\ts_nop 0\n\tbuffer_load_dword %1, %2, %3 offen lds" ::"s"(lds), "v"(voff), "s"(srd), "s"(soff) : "m0", "memory");
}
#define WAITBAR(n) asm volatile("s_waitcnt vmcnt(" #n ") lgkmcnt(0)\n\ts_barrier" ::: "memory")
#define LBAR() asm volatile("s_waitcnt lgkmcnt(0)\n\ts_barrier" ::: "memory")
#define LWAIT() asm volatile("s_waitcnt lgkmcnt(0)" ::: "memory")
DI v4u pack8(const f16v& a, int o) {
  return v4u{pk2(a[o], a[o + 1]), pk2(a[o + 2], a[o + 3]), pk2(a[o + 4], a[o + 5]), pk2(a[o + 6], a[o + 7])};
}
DI __amdgpu_buffer_rsrc_t mkrsrc(const void* p, u32 bytes) {
  return __builtin_amdgcn_make_buffer_rsrc((void*)p, 0, bytes, 0x00020000);
}

DI void dual_softmax(const f16v& acc, int o, v4u& pos, v4u& neg) {
  constexpr float L = 1.4426950408889634f;
  float p[8];
#pragma unroll
  for (int i = 0; i < 4; ++i) { u32 u = pk2(acc[o + 2 * i], acc[o + 2 * i + 1]); p[2 * i] = blo(u); p[2 * i + 1] = bhi(u); }
  float mx = fmaxf(fmaxf(fmaxf(p[0], p[1]), fmaxf(p[2], p[3])), fmaxf(fmaxf(p[4], p[5]), fmaxf(p[6], p[7])));
  float mn = fminf(fminf(fminf(p[0], p[1]), fminf(p[2], p[3])), fminf(fminf(p[4], p[5]), fminf(p[6], p[7])));
  {
    auto r = __builtin_amdgcn_permlane32_swap(__float_as_uint(mx), __float_as_uint(-mn), false, false);
    float m = fmaxf(__uint_as_float(r[0]), __uint_as_float(r[1]));
    auto r2 = __builtin_amdgcn_permlane32_swap(__float_as_uint(m), __float_as_uint(m), false, false);
    mx = __uint_as_float(r2[0]); mn = -__uint_as_float(r2[1]);
  }
  const float cp = -mx * L, cn = mn * L;
  float ep[8], en[8];
#pragma unroll
  for (int i = 0; i < 8; ++i) {
    ep[i] = __builtin_amdgcn_exp2f(__builtin_fmaf(p[i], L, cp));
    en[i] = __builtin_amdgcn_exp2f(__builtin_fmaf(p[i], -L, cn));
  }
  float sp = ((ep[0] + ep[1]) + (ep[2] + ep[3])) + ((ep[4] + ep[5]) + (ep[6] + ep[7]));
  float sn = ((en[0] + en[1]) + (en[2] + en[3])) + ((en[4] + en[5]) + (en[6] + en[7]));
  {
    auto r = __builtin_amdgcn_permlane32_swap(__float_as_uint(sp), __float_as_uint(sn), false, false);
    float m = __builtin_amdgcn_rcpf(__uint_as_float(r[0]) + __uint_as_float(r[1]));
    auto r2 = __builtin_amdgcn_permlane32_swap(__float_as_uint(m), __float_as_uint(m), false, false);
    sp = __uint_as_float(r2[0]); sn = __uint_as_float(r2[1]);
  }
  pos = v4u{pk2(ep[0] * sp, ep[1] * sp), pk2(ep[2] * sp, ep[3] * sp), pk2(ep[4] * sp, ep[5] * sp), pk2(ep[6] * sp, ep[7] * sp)};
  neg = v4u{pk2(en[0] * sn, en[1] * sn), pk2(en[2] * sn, en[3] * sn), pk2(en[4] * sn, en[5] * sn), pk2(en[6] * sn, en[7] * sn)};
}

constexpr int SLOT = 24576;
DI void attn_block(int b, const char* x, const char* wqkv, const char* wphi, char* ws, LAS char* sm) {
  STAMP_DECL; STAMP(0);
  const int tid = threadIdx.x, lane = tid & 63, w = __builtin_amdgcn_readfirstlane(tid >> 6);
  const int xcd = b & 7, j = b >> 3, a = j & 7, c = j >> 3;
  const int rt = (xcd >> 1) * 4 + c, hg = (xcd & 1) * 8 + a;
  const int rot = ((a & 1) + (c & 1) * 2) * 2;
  const u32 tag = *(const CAS u32*)(ws + WS_EPOCH + hg * 256) + 1u;
  const int h = lane >> 5, r32 = lane & 31;
  const int G = lane >> 4, q4 = (lane & 15) >> 2, p4 = lane & 3, blk = G & 1;

  const v4u xd = mksrd(x, 2u << 20), wd = mksrd(wqkv, 1536u * 1024u), pd = mksrd(wphi, 16384u);
  const u32 lb = (u32)(size_t)sm;
  const u32 xb = (rt * 128 + w * 32) * 1024;
  const u32 xo0 = (lane >> 3) * 1024 + (((lane & 7) ^ (lane >> 4)) << 4);
  const u32 xo1 = xo0 ^ 64;
  const u32 wb = hg * 64 + 16 * w * 3072;
  const u32 wo = (lane >> 2) * 3072 + (lane & 3) * 16;
  auto stage = [&](int s0) {
    const int s = (s0 + rot) & 7;
    const u32 base = lb + (s0 % 3) * SLOT;
    const u32 xs = xb + s * 128, wsb = wb + s * (64 * 3072);
#pragma unroll
    for (int i = 0; i < 4; ++i) dma16(xd, (i & 1) ? xo1 : xo0, xs + i * 8192, base + w * 4096 + i * 1024);
    dma16(wd, wo, wsb + 1024, base + 16384 + w * 1024);
    dma16(wd, wo, wsb + 2048, base + 20480 + w * 1024);
  };
  auto stage_q = [&](int s0) {
    const int s = (s0 + rot) & 7;
    dma16(wd, wo, wb + s * (64 * 3072), lb + 2 * SLOT + s0 * 4096 + w * 1024);
  };
  dma4(pd, tid * 4, hg * 1024, lb + 3 * SLOT + w * 256);
  stage(0); stage(1); stage(2);

  LAS char* xr[4];
#pragma unroll
  for (int ks = 0; ks < 4; ++ks) xr[ks] = sm + w * 4096 + r32 * 128 + (((2 * ks + h) ^ ((r32 >> 1) & 7)) << 4);
  LAS char* wr = sm + (8 * h + q4) * 64 + blk * 32 + p4 * 8;

  v4u xf[32];
  v4u wph;
  f16v ak = {}, av = {};
#pragma unroll
  for (int s0 = 0; s0 < 8; ++s0) {
    if (s0 == 0) WAITBAR(12);
    else if (s0 == 2 || s0 == 3) WAITBAR(8);
    else WAITBAR(6);
    if (s0 == 0) STAMP(1);
    if (s0 == 2) STAMP(8);
    if (s0 == 4) STAMP(9);
    if (s0 == 6) STAMP(10);
    if (s0 == 7) STAMP(11);
    if (s0 >= 1 && s0 <= 5) stage(s0 + 2);
    if (s0 == 1) { stage_q(6); stage_q(7); }
    if (s0 == 6) {
#pragma unroll
      for (int i = 0; i < 6; ++i) stage_q(i);
    }
    if (s0 == 0) {
      LAS char* pp = sm + 3 * SLOT + blk * 512 + (4 * h + q4) * 32 + p4 * 8;
      wph = trd2(pp, pp + 256);
    }
    const int so = (s0 % 3) * SLOT;
#pragma unroll
    for (int ks = 0; ks < 4; ++ks) {
      xf[s0 * 4 + ks] = ld128(xr[ks] + so);
      v4u fk = trd2(wr + so + 16384 + ks * 1024, wr + so + 16384 + ks * 1024 + 256);
      v4u fv = trd2(wr + so + 20480 + ks * 1024, wr + so + 20480 + ks * 1024 + 256);
      ak = mfma32(fk, xf[s0 * 4 + ks], ak);
      av = mfma32(xf[s0 * 4 + ks], fv, av);
    }
  }
  STAMP(2);
  const v4u zero4 = {0, 0, 0, 0};
  const v4u wp0 = blk == 0 ? wph : zero4, wp1 = blk == 0 ? zero4 : wph;
  {
    f16v pk = {};
    pk = mfma32(wp0, pack8(ak, 0), pk);
    pk = mfma32(wp1, pack8(ak, 8), pk);
    LAS char* img = sm + SLOT + w * 4096;
    const int sw = (r32 >> 1) & 7;
#pragma unroll
    for (int hd = 0; hd < 2; ++hd) {
      v4u pos, neg;
      dual_softmax(pk, 8 * hd, pos, neg);
      LAS char* row = img + hd * 2048 + r32 * 64;
      *(LAS v2u*)(row + ((h ^ sw) << 3)) = v2u{pos.x, pos.y};
      *(LAS v2u*)(row + (((2 + h) ^ sw) << 3)) = v2u{pos.z, pos.w};
      *(LAS v2u*)(row + (((4 + h) ^ sw) << 3)) = v2u{neg.x, neg.y};
      *(LAS v2u*)(row + (((6 + h) ^ sw) << 3)) = v2u{neg.z, neg.w};
    }
    LWAIT();
    const v4u ones = {0x3f803f80u, 0x3f803f80u, 0x3f803f80u, 0x3f803f80u};
    const v4u vf0 = pack8(av, 0), vf1 = pack8(av, 8);
    LAS char* scr = sm + w * 5120 + r32 * 80 + h * 16;
#pragma unroll
    for (int hd = 0; hd < 2; ++hd) {
      f16v kv = {};
#pragma unroll
      for (int s = 0; s < 2; ++s) {
        const int t0 = 16 * s + 4 * h + q4, t1 = t0 + 8;
        v4u bfr = trd2(img + hd * 2048 + t0 * 64 + (((4 * blk + p4) ^ ((t0 >> 1) & 7)) << 3),
                       img + hd * 2048 + t1 * 64 + (((4 * blk + p4) ^ ((t1 >> 1) & 7)) << 3));
        v4u afr = (blk == hd) ? (s ? vf1 : vf0) : ones;
        kv = mfma32(afr, bfr, kv);
      }
      const int o = 8 * hd;
      *(LAS f4*)(scr + hd * 2560) = f4{kv[o], kv[o + 1], kv[o + 2], kv[o + 3]};
      *(LAS f4*)(scr + hd * 2560 + 32) = f4{kv[o + 4], kv[o + 5], kv[o + 6], kv[o + 7]};
      if (h == 0) *(LAS float*)(scr + hd * 2560 + 64) = kv[8 - o];
    }
  }
  WAITBAR(0);
  const __amdgpu_buffer_rsrc_t kvr = mkrsrc(ws + WS_KVP + (size_t)hg * 16 * KVP_STRIDE, 16 * KVP_STRIDE);
  {
    LAS char* p = sm + (tid >> 2) * 80 + (tid & 3) * 16;
    f4 s = (*(LAS f4*)p + *(LAS f4*)(p + 5120)) + (*(LAS f4*)(p + 10240) + *(LAS f4*)(p + 15360));
    __builtin_amdgcn_raw_buffer_store_b128(v4u{pk2(s[0], s[1]), tag, pk2(s[2], s[3]), tag}, kvr, rt * KVP_STRIDE + tid * 16, 0, 16);
    if (tid < 32) {
      LAS char* z = sm + tid * 160 + 64;
      float z0 = (*(LAS float*)z + *(LAS float*)(z + 5120)) + (*(LAS float*)(z + 10240) + *(LAS float*)(z + 15360));
      z += 80;
      float z1 = (*(LAS float*)z + *(LAS float*)(z + 5120)) + (*(LAS float*)(z + 10240) + *(LAS float*)(z + 15360));
      __builtin_amdgcn_raw_buffer_store_b128(v4u{__float_as_uint(z0), tag, __float_as_uint(z1), tag}, kvr,
                                             rt * KVP_STRIDE + 4096 + tid * 16, 0, 16);
    }
  }
  STAMP(3);
  f16v aq = {};
#pragma unroll
  for (int s0 = 0; s0 < 8; ++s0)
#pragma unroll
    for (int ks = 0; ks < 4; ++ks) {
      v4u fq = trd2(wr + 2 * SLOT + s0 * 4096 + ks * 1024, wr + 2 * SLOT + s0 * 4096 + ks * 1024 + 256);
      aq = mfma32(fq, xf[s0 * 4 + ks], aq);
    }
  STAMP(4);
  v4u rec[16], zr[4];
  const u32 ro = tid * 16, zo = (tid & 3) * 4 * KVP_STRIDE + 4096 + (tid >> 3) * 16;
  auto sweep = [&]() {
#pragma unroll
    for (int p = 0; p < 16; ++p) rec[p] = __builtin_amdgcn_raw_buffer_load_b128(kvr, ro, p * KVP_STRIDE, 16);
#pragma unroll
    for (int p = 0; p < 4; ++p) zr[p] = __builtin_amdgcn_raw_buffer_load_b128(kvr, zo, p * KVP_STRIDE, 16);
  };
  __builtin_amdgcn_sched_barrier(0);
  sweep();
  __builtin_amdgcn_sched_barrier(0);
  v4u qp[2], qn[2];
  {
    f16v pq = {};
    pq = mfma32(wp0, pack8(aq, 0), pq);
    pq = mfma32(wp1, pack8(aq, 8), pq);
    dual_softmax(pq, 0, qp[0], qn[0]);
    dual_softmax(pq, 8, qp[1], qn[1]);
  }
  STAMP(5);
  for (u32 spins = 0;; ++spins) {
    u32 bad = 0;
#pragma unroll
    for (int p = 0; p < 16; ++p) bad |= (rec[p].y ^ tag) | (rec[p].w ^ tag);
#pragma unroll
    for (int p = 0; p < 4; ++p) bad |= (zr[p].y ^ tag) | (zr[p].w ^ tag);
    if (!__any(bad != 0) || spins >= SPIN_CAP) break;
    __builtin_amdgcn_s_sleep(2);
    asm volatile("" ::: "memory");
    sweep();
  }
  STAMP(6);
  {
    float s0 = 0, s1 = 0, s2 = 0, s3 = 0, z = 0;
#pragma unroll
    for (int p = 0; p < 16; ++p) { s0 += blo(rec[p].x); s1 += bhi(rec[p].x); s2 += blo(rec[p].z); s3 += bhi(rec[p].z); }
#pragma unroll
    for (int p = 0; p < 4; ++p) z += __uint_as_float((tid & 4) ? zr[p].z : zr[p].x);
    z += __uint_as_float(__builtin_amdgcn_mov_dpp(__float_as_uint(z), 0xB1, 0xf, 0xf, true));
    z += __uint_as_float(__builtin_amdgcn_mov_dpp(__float_as_uint(z), 0x4E, 0xf, 0xf, true));
    LAS char* p = sm + SLOT + 16384 + (tid >> 2) * 64 + (tid & 3) * 8;
    const u32 zz = pk2(z, z);
    *(LAS v2u*)p = v2u{pk2(s0, s1), pk2(s2, s3)};
    *(LAS v2u*)(p + 32) = v2u{zz, zz};
  }
  LBAR();
  if (rt == 0 && tid == 0) __hip_atomic_store((GAS u32*)(ws + WS_EPOCH + hg * 256), tag, __ATOMIC_RELAXED, __HIP_MEMORY_SCOPE_AGENT);
  {
    LAS char* im = sm + SLOT + 16384 + (4 * h + q4) * 64 + blk * 32 + p4 * 8;
    char* ob = ws + WS_O + ((size_t)(rt * 4 + w) * 32 + hg * 2) * 1024 + lane * 16;
#pragma unroll
    for (int hd = 0; hd < 2; ++hd) {
      f16v nm = {};
      nm = mfma32(trd2(im + hd * 2048, im + hd * 2048 + 512), qp[hd], nm);
      nm = mfma32(trd2(im + hd * 2048 + 1024, im + hd * 2048 + 1536), qn[hd], nm);
      const float inv = __builtin_amdgcn_rcpf(rbf(rbf(nm[8]) + 1e-6f));
      v4u nb = pack8(nm, 0);
      v4u o = {pk2(blo(nb.x) * inv, bhi(nb.x) * inv), pk2(blo(nb.y) * inv, bhi(nb.y) * inv),
               pk2(blo(nb.z) * inv, bhi(nb.z) * inv), pk2(blo(nb.w) * inv, bhi(nb.w) * inv)};
      *(GAS v4u*)(ob + hd * 1024) = o;
    }
  }
  STAMP(7);
  STAMP_FLUSH(b);
}

DI f2 cmul(f2 a, f2 b) { return f2{a.x, a.x} * b + f2{-a.y, a.y} * f2{b.y, b.x}; }
DI f2 cexp2(float lr, float li, float t) {
  const float e = __builtin_amdgcn_exp2f(lr * t), ph = __builtin_amdgcn_fractf(li * t);
  return f2{e, e} * f2{__builtin_amdgcn_cosf(ph), __builtin_amdgcn_sinf(ph)};
}
DI u32 kc_index(int lag) {
  const int v = (int)rbf((float)lag);
  const int col = v & 0xf0, lo = (v >> 2) & 3;
  const int i1 = 256 + col + ((v & 2) ? 12 : 0) + lo;
  const int i2 = 256 + col + 4 + ((v >> 6) & 4) + lo;
  const int i3 = 512 + col + ((v & 8) ? 12 : 0) + ((v >> 8) & 3);
  int r = v < 256 ? v : v < 512 ? i1 : v < 1024 ? i2 : v < 2048 ? i3 : 516;
  return lag < 0 ? 768 : r;
}
constexpr int S_TBL = 0, TBL_CH = 8448, TBL_ODD = 4160;
constexpr int S_RED = 32768;
constexpr int S_U = 65536, U_CH = 4608;
constexpr int S_KC = 75776, KC_CH = 2048;

DI void ssm_block(int s, const char* x, const char* Are, const char* Aim, const char* Bre, const char* Bim,
                  const char* Cre, const char* Cim, const char* logdt, const char* dsk, const char* gw, char* ws,
                  LAS char* sm) {
  STAMP_DECL; STAMP(0);
  const int tid = threadIdx.x, lane = tid & 63, w = __builtin_amdgcn_readfirstlane(tid >> 6);
  const int grp = s >> 5, ta = s & 31;
  const u32 tag = *(const CAS u32*)(ws + WS_EPOCH + (16 + grp) * 256) + 1u;
  const int G = lane >> 4, q4 = (lane & 15) >> 2, p4 = lane & 3;
  const int ch = w >> 1, type = (w & 1) * 2 + (lane >> 5), q = lane & 31;
  const char* xt = x + (size_t)(ta * 64 + (tid >> 3)) * 1024 + grp * 128 + (tid & 7) * 16;
  const v4u t0 = *(const GAS v4u*)xt, t1 = *(const GAS v4u*)(xt + 32768);
  const u32 po = (2 * s + ch) * 256 + q * 8;
  const v2u are = *(const GAS v2u*)(Are + po), aim = *(const GAS v2u*)(Aim + po);
  const v2u bre = *(const GAS v2u*)(Bre + po), bim = *(const GAS v2u*)(Bim + po);
  const v2u cre = *(const GAS v2u*)(Cre + po), cim = *(const GAS v2u*)(Cim + po);
  const u32 ldt2 = *(const CAS u32*)(logdt + s * 4), dsk2 = *(const CAS u32*)(dsk + s * 4);
  const char* gx = x + (size_t)(s * 8 + w * 2) * 1024 + lane * 16;
  const v4u g0 = *(const GAS v4u*)gx, g1 = *(const GAS v4u*)(gx + 1024), gwv = *(const GAS v4u*)(gw + lane * 16);
  u32 ki[9];
#pragma unroll
  for (int e = 0; e < 9; ++e) ki[e] = kc_index(2047 - 8 * tid - e) * 2;
  {
    const int r0 = tid >> 3, r1 = r0 + 32, cc = tid & 7;
    *(LAS v4u*)(sm + S_U + r0 * 128 + ((cc ^ (2 * ((r0 >> 1) & 3))) << 4)) = t0;
    *(LAS v4u*)(sm + S_U + r1 * 128 + ((cc ^ (2 * ((r1 >> 1) & 3))) << 4)) = t1;
    if (tid < 2) *(LAS u32*)(sm + S_KC + tid * KC_CH + 1536) = 0;
    LBAR();
    STAMP(1);
    LAS char* im2 = sm + w * 2048;
    const int ci = lane & 15;
#pragma unroll
    for (int k = 0; k < 4; ++k) {
      const int t = 16 * G + 4 * k + q4;
      v2u v = trd(sm + S_U + t * 128 + (((2 * w + (p4 >> 1)) ^ (2 * ((t >> 1) & 3))) << 4) + (p4 & 1) * 8);
      *(LAS v2u*)(im2 + ci * 128 + (((4 * G + k) ^ ci) << 3)) = v;
    }
    LWAIT();
    const __amdgpu_buffer_rsrc_t xr = mkrsrc(ws + WS_XT, 4u << 20);
#pragma unroll
    for (int k = 0; k < 4; ++k) {
      const int c = 4 * k + G;
      v2u v = *(LAS v2u*)(im2 + c * 128 + ((ci ^ c) << 3));
      __builtin_amdgcn_raw_buffer_store_b128(v4u{v.x, tag, v.y, tag}, xr, (grp * 64 + w * 16 + c) * 8192 + ta * 256 + ci * 16, 0, 16);
    }
  }
  {
    float a0 = 0, a1 = 0;
#pragma unroll
    for (int i = 0; i < 4; ++i) {
      u32 u0 = pk2(blo(g0[i]) * blo(gwv[i]), bhi(g0[i]) * bhi(gwv[i]));
      u32 u1 = pk2(blo(g1[i]) * blo(gwv[i]), bhi(g1[i]) * bhi(gwv[i]));
      a0 += blo(u0) + bhi(u0); a1 += blo(u1) + bhi(u1);
    }
    auto r = __builtin_amdgcn_permlane32_swap(__float_as_uint(a0), __float_as_uint(a1), false, false);
    float v = __uint_as_float(r[0]) + __uint_as_float(r[1]);
#pragma unroll
    for (int m = 16; m >= 1; m >>= 1) v += __shfl_xor(v, m);
    v = rbf(v);
    v = rbf(__builtin_amdgcn_rcpf(1.f + __builtin_amdgcn_exp2f(-1.4426950408889634f * v)));
    if ((lane & 31) == 0) *(GAS float*)(ws + WS_G + (s * 8 + w * 2 + (lane >> 5)) * 4) = v;
  }
  STAMP(2);
  {
    const float dt = rbf(expf(ch ? bhi(ldt2) : blo(ldt2)));
    const bool isA = type == 0;
    const float tG = type < 2 ? 0.f : type == 2 ? 256.f : 1024.f;
    const float tP = type == 0 ? 192.f : type == 1 ? 12.f : type == 2 ? 2.f : 8.f;
    const float tQ = type == 0 ? 64.f : type == 1 ? 4.f : type == 2 ? 256.f : 1024.f;
    const float tR = type == 0 ? 16.f : type == 1 ? 1.f : type == 2 ? 4.f : 256.f;
    const float si = isA ? -0.15915494309189535f : 0.15915494309189535f;
    u32 ev[16][4];
#pragma unroll
    for (int n = 0; n < 4; ++n) {
      auto el = [&](const v2u& v) { return (n & 1) ? bhi(v[n >> 1]) : blo(v[n >> 1]); };
      const float lr = (dt * el(are)) * 1.4426950408889634f, li = (dt * el(aim)) * si;
      const float br = dt * el(bre), bi = dt * el(bim), cr = el(cre), ci = el(cim);
      const f2 W = {isA ? cr * br - ci * bi : 1.f, isA ? -(cr * bi + ci * br) : 0.f};
      f2 g[4], r[4];
      g[0] = cmul(cexp2(lr, li, tG), W);
      const f2 P = cexp2(lr, li, tP), Q = cexp2(lr, li, tQ);
      r[1] = cexp2(lr, li, tR);
      g[3] = cmul(g[0], P); g[1] = cmul(g[0], Q); g[2] = cmul(g[1], Q);
      r[2] = cmul(r[1], r[1]); r[3] = cmul(r[2], r[1]);
#pragma unroll
      for (int gi = 0; gi < 4; ++gi) {
        ev[4 * gi][n] = pk2(g[gi].x, g[gi].y);
#pragma unroll
        for (int ri = 1; ri < 4; ++ri) { const f2 v = cmul(g[gi], r[ri]); ev[4 * gi + ri][n] = pk2(v.x, v.y); }
      }
    }
    LBAR();
    LAS char* eb = sm + (ch * 4 + type) * 8192 + q * 256;
#pragma unroll
    for (int sl = 0; sl < 16; ++sl) *(LAS v4u*)(eb + ((sl ^ (q & 15)) << 4)) = v4u{ev[sl][0], ev[sl][1], ev[sl][2], ev[sl][3]};
  }
  LBAR();
  STAMP(3);
  const __amdgpu_buffer_rsrc_t ur = mkrsrc(ws + WS_XT + (size_t)s * 16384, 16384);
  v4u ur4[4];
  auto sweep = [&]() {
#pragma unroll
    for (int k = 0; k < 4; ++k) ur4[k] = __builtin_amdgcn_raw_buffer_load_b128(ur, tid * 16, k * 4096, 16);
  };
  sweep();
  {
    const int c2 = w & 1, r = lane & 15;
    LAS char* eb = sm + c2 * 32768 + G * 256;
    const int tl = w < 2 ? 0 : 1;
    f4 k0 = {}, k1 = {};
#pragma unroll
    for (int ks = 0; ks < 8; ++ks) {
      const int off = ks * 1024 + ((r ^ ((4 * ks + G) & 15)) << 4);
      const v4u fa = ld128(eb + off);
      k0 = mfma16(ld128(eb + (1 + tl) * 8192 + off), fa, k0);
      if (w < 2) k1 = mfma16(ld128(eb + 3 * 8192 + off), fa, k1);
    }
    LAS char* kc = sm + S_KC + c2 * KC_CH + r * 32 + G * 8;
    *(LAS v2u*)(kc + tl * 512) = v2u{pk2(k0[0], k0[1]), pk2(k0[2], k0[3])};
    if (w < 2) *(LAS v2u*)(kc + 1024) = v2u{pk2(k1[0], k1[1]), pk2(k1[2], k1[3])};
  }
  LBAR();
  STAMP(4);
#pragma unroll
  for (int c2 = 0; c2 < 2; ++c2) {
    u32 kv[9];
#pragma unroll
    for (int e = 0; e < 9; ++e) kv[e] = *(LAS u16*)(sm + S_KC + c2 * KC_CH + ki[e]);
    LAS char* tb = sm + S_TBL + c2 * TBL_CH + tid * 16;
    *(LAS v4u*)tb = v4u{kv[0] | (kv[1] << 16), kv[2] | (kv[3] << 16), kv[4] | (kv[5] << 16), kv[6] | (kv[7] << 16)};
    *(LAS v4u*)(tb + TBL_ODD) = v4u{kv[1] | (kv[2] << 16), kv[3] | (kv[4] << 16), kv[5] | (kv[6] << 16), kv[7] | (kv[8] << 16)};
  }
  if (tid < 8) *(LAS v4u*)(sm + S_TBL + (tid >> 2) * TBL_CH + ((tid >> 1) & 1) * TBL_ODD + 4096 + (tid & 1) * 16) = v4u{0, 0, 0, 0};
  if (tid >= 64 && tid < 124) {
    const int i = tid - 64;
    *(LAS v4u*)(sm + S_U + (i >= 30 ? U_CH + (i - 30) * 16 : i * 16)) = v4u{0, 0, 0, 0};
  }
  for (u32 spins = 0;; ++spins) {
    u32 bad = 0;
#pragma unroll
    for (int k = 0; k < 4; ++k) bad |= (ur4[k].y ^ tag) | (ur4[k].w ^ tag);
    if (!__any(bad != 0) || spins >= SPIN_CAP) break;
    __builtin_amdgcn_s_sleep(2);
    asm volatile("" ::: "memory");
    sweep();
  }
#pragma unroll
  for (int k = 0; k < 4; ++k)
    *(LAS v2u*)(sm + S_U + (k >> 1) * U_CH + 480 + ((k & 1) * 256 + tid) * 8) = v2u{ur4[k].x, ur4[k].z};
  LBAR();
  STAMP(5);
  if (ta == 0 && tid == 0) __hip_atomic_store((GAS u32*)(ws + WS_EPOCH + (16 + grp) * 256), tag, __ATOMIC_RELAXED, __HIP_MEMORY_SCOPE_AGENT);
  const int rh = w & 1, m = lane & 15;
  f4 acc[8] = {};
  {
    LAS char* ub = sm + S_U + ch * U_CH + m * 32 + G * 16 + (3 - 4 * rh) * 64 + 64;
    const int s0 = 2031 + 8 * G - m;
    LAS char* kb = sm + S_TBL + ch * TBL_CH + ((s0 & 1) ? TBL_ODD : 0) + (s0 >> 1) * 4 - rh * 256 - 59 * 64;
#pragma unroll
    for (int rr = 0; rr < 4; ++rr) {
      v4u bf[8];
#pragma unroll
      for (int i = 0; i < 8; ++i) bf[i] = ld128(ub + i * 512 + (3 - rr) * 64);
#pragma unroll
      for (int J = 0; J < 8; ++J) {
        LAS u32* kp = (LAS u32*)(kb + (59 - 8 * J - rr) * 64);
        const v4u af = {kp[0], kp[1], kp[2], kp[3]};
#pragma unroll
        for (int i = J; i < 8; ++i) acc[i] = mfma16(af, bf[i - J], acc[i]);
      }
    }
  }
  STAMP(6);
  {
    LAS char* mine = sm + S_RED + w * 4096 + lane * 16;
    LAS char* other = sm + S_RED + (w ^ 1) * 4096 + lane * 16;
#pragma unroll
    for (int i = 0; i < 4; ++i) {
      f4 give;
#pragma unroll
      for (int e = 0; e < 4; ++e) { const float lo = acc[i][e], hi = acc[4 + i][e]; give[e] = rh ? lo : hi; acc[i][e] = rh ? hi : lo; }
      *(LAS f4*)(mine + i * 1024) = give;
    }
    LBAR();
    const float dk = ch ? bhi(dsk2) : blo(dsk2);
    LAS char* up = sm + S_U + ch * U_CH + 480 + rh * 2048 + m * 32 + G * 8;
    char* bo = ws + WS_BT + (size_t)(2 * s + ch) * 4096 + rh * 2048 + m * 32 + G * 8;
#pragma unroll
    for (int i = 0; i < 4; ++i) {
      const f4 y = acc[i] + *(LAS f4*)(other + i * 1024);
      const v2u u = *(LAS v2u*)(up + i * 512);
      const u32 y0 = pk2(y[0], y[1]), y1 = pk2(y[2], y[3]);
      const u32 d0 = pk2(blo(u.x) * dk, bhi(u.x) * dk), d1 = pk2(blo(u.y) * dk, bhi(u.y) * dk);
      *(GAS v2u*)(bo + i * 512) = v2u{pk2(blo(y0) + blo(d0), bhi(y0) + bhi(d0)), pk2(blo(y1) + blo(d1), bhi(y1) + bhi(d1))};
    }
  }
  STAMP(7);
  STAMP_FLUSH(256 + s);
}

__global__ __launch_bounds__(256, 2) void k_front(const char* x, const char* wqkv, const char* wphi, const char* Are,
                                                   const char* Aim, const char* Bre, const char* Bim, const char* Cre,
                                                   const char* Cim, const char* logdt, const char* dsk, const char* gw,
                                                   char* ws) {
  extern __shared__ __attribute__((aligned(16))) char smem[];
  LAS char* sm = (LAS char*)smem;
  if (blockIdx.x < 256) attn_block(blockIdx.x, x, wqkv, wphi, ws, sm);
  else ssm_block(blockIdx.x - 256, x, Are, Aim, Bre, Bim, Cre, Cim, logdt, dsk, gw, ws, sm);
}

constexpr int B_W = 65536, B_B = 131072, B_G = 139264, B_LDS = 140288;
__global__ __launch_bounds__(256, 1) void k_back(const char* wo, const char* ws, char* out) {
  extern __shared__ __attribute__((aligned(16))) char smem[];
  LAS char* sm = (LAS char*)smem;
  STAMP_DECL; STAMP(0);
  const int tid = threadIdx.x, lane = tid & 63, w = __builtin_amdgcn_readfirstlane(tid >> 6);
  const int b = blockIdx.x, xcd = b & 7, j = b >> 3;
  const int tb = (xcd >> 1) * 8 + (j >> 2), cb = (xcd & 1) * 4 + (j & 3);
  const int rot = (j + (j >> 2)) & 3;
  const int wc = w & 1, wt = w >> 1;
  const int h = lane >> 5, G = lane >> 4, q4 = (lane & 15) >> 2, p4 = lane & 3, blk = G & 1;
  const u32 lo = lane * 16;
  const u32 so = (lane >> 3) * 1024 + (((lane & 7) ^ (((lane >> 4) & 1) << 2)) << 4);
  const v4u sd = mksrd(ws, 10u << 20), wd = mksrd(wo, 512u * 1024u);
  const u32 lb = (u32)(size_t)sm;
  const u32 ob = (u32)WS_O + ((tb * 2 + (w >> 1)) * 32 + (w & 1) * 4) * 1024;
  const u32 wb = cb * 128 + w * 32 * 1024;
#pragma unroll
  for (int s0 = 0; s0 < 4; ++s0) {
    const int s = (s0 + rot) & 3;
#pragma unroll
    for (int i = 0; i < 4; ++i) dma16(sd, lo, ob + (s * 8 + i) * 1024, lb + s0 * 16384 + w * 4096 + i * 1024);
#pragma unroll
    for (int i = 0; i < 4; ++i) dma16(wd, so, wb + s * 131072 + i * 8192, lb + B_W + s0 * 16384 + w * 4096 + i * 1024);
  }
  {
    const u32 bo = (lane >> 3) * 4096 + (((lane & 7) ^ (((lane >> 4) & 1) << 2)) << 4);
    const u32 bb = (u32)WS_BT + (cb * 64 + w * 16) * 4096 + tb * 128;
    dma16(sd, bo, bb, lb + B_B + w * 2048);
    dma16(sd, bo, bb + 8 * 4096, lb + B_B + w * 2048 + 1024);
    dma4(sd, lane * 4, (u32)WS_G + tb * 256, lb + B_G + w * 256);
  }
  const int sx = (4 * wc + 2 * blk + (p4 >> 1)) ^ ((q4 >> 1) << 2);
  LAS char* wr = sm + B_W + (4 * h + q4) * 128 + sx * 16 + (p4 & 1) * 8;
  LAS char* orr = sm + wt * 8192 + lo;
  f16v acc = {};
#pragma unroll
  for (int s0 = 0; s0 < 4; ++s0) {
    if (s0 == 0) WAITBAR(27);
    else if (s0 == 1) WAITBAR(19);
    else if (s0 == 2) WAITBAR(11);
    else WAITBAR(0);
    if (s0 == 0) STAMP(1);
#pragma unroll
    for (int hh = 0; hh < 8; ++hh)
      acc = mfma32(trd2(wr + s0 * 16384 + hh * 2048, wr + s0 * 16384 + hh * 2048 + 1024), ld128(orr + s0 * 16384 + hh * 1024), acc);
  }
  STAMP(2);
  const int t = wt * 32 + (lane & 31);
  const float g = *(LAS float*)(sm + B_G + t * 4), g1 = rbf(1.f - g);
  const int bx = (4 * wt + 2 * blk + (p4 >> 1)) ^ ((q4 >> 1) << 2);
  LAS char* br = sm + B_B + (32 * wc + 4 * h + q4) * 128 + bx * 16 + (p4 & 1) * 8;
  v2u o[4];
#pragma unroll
  for (int k = 0; k < 4; ++k) {
    const v2u bv = trd(br + k * 1024);
    const u32 a0 = pk2(acc[4 * k], acc[4 * k + 1]), a1 = pk2(acc[4 * k + 2], acc[4 * k + 3]);
    const u32 m0 = pk2(g * blo(a0), g * bhi(a0)), m1 = pk2(g * blo(a1), g * bhi(a1));
    const u32 n0 = pk2(g1 * blo(bv.x), g1 * bhi(bv.x)), n1 = pk2(g1 * blo(bv.y), g1 * bhi(bv.y));
    o[k] = v2u{pk2(blo(m0) + blo(n0), bhi(m0) + bhi(n0)), pk2(blo(m1) + blo(n1), bhi(m1) + bhi(n1))};
  }
  char* op = out + (size_t)(tb * 64 + t) * 1024 + cb * 128 + wc * 64 + h * 16;
#pragma unroll
  for (int k = 0; k < 4; k += 2) {
    auto rx = __builtin_amdgcn_permlane32_swap(o[k].x, o[k + 1].x, false, false);
    auto ry = __builtin_amdgcn_permlane32_swap(o[k].y, o[k + 1].y, false, false);
    *(GAS v4u*)(op + k * 16) = v4u{rx[0], ry[0], rx[1], ry[1]};
  }
  STAMP(3);
  STAMP_FLUSH(512 + b);
}

extern "C" void kernel_launch(void* const* d_in, const int* in_sizes, int n_in, void* d_out, int out_size, void* d_ws,
                              size_t ws_size, hipStream_t stream) {
  (void)in_sizes; (void)n_in; (void)out_size; (void)ws_size;
  (void)hipFuncSetAttribute((const void*)k_front, hipFuncAttributeMaxDynamicSharedMemorySize, 81920);
  (void)hipFuncSetAttribute((const void*)k_back, hipFuncAttributeMaxDynamicSharedMemorySize, B_LDS);
  const char* const* in = (const char* const*)d_in;
  k_front<<<512, 256, 81920, stream>>>(in[0], in[1], in[2], in[5], in[6], in[7], in[8], in[9], in[10], in[11], in[12],
                                       in[13], (char*)d_ws);
  k_back<<<256, 256, B_LDS, stream>>>(in[3], (const char*)d_ws, (char*)d_out);
}
```

```cpp
#include <hip/hip_runtime.h>
#include <stdint.h>
#if defined(__HIP_DEVICE_COMPILE__) && __HIP_DEVICE_COMPILE__
#pragma clang attribute push(__attribute__((target("no-packed-fp32-ops"))), apply_to = function)
#define NOPK_POP _Pragma("clang attribute pop")
#else
#define NOPK_POP
#endif

typedef unsigned short u16;
typedef unsigned int u32;
typedef unsigned long long u64;
typedef short s16x4 __attribute__((ext_vector_type(4)));
typedef __bf16 bf2 __attribute__((ext_vector_type(2)));
typedef __bf16 bf8 __attribute__((ext_vector_type(8)));
typedef float f2 __attribute__((ext_vector_type(2)));
typedef float f4 __attribute__((ext_vector_type(4)));
typedef float f16v __attribute__((ext_vector_type(16)));
typedef u32 v2u __attribute__((ext_vector_type(2)));
typedef u32 v4u __attribute__((ext_vector_type(4)));
#define LAS __attribute__((address_space(3)))
#define GAS __attribute__((address_space(1)))
#define CAS __attribute__((address_space(4)))
#define DI __device__ __forceinline__
#define TID ((int)__builtin_amdgcn_workitem_id_x())
#define BID ((int)__builtin_amdgcn_workgroup_id_x())
#define PRIO(n) __builtin_amdgcn_s_setprio(n)
#define SEXIT 99
#define SX(n) if (SEXIT == n) { asm volatile("s_waitcnt vmcnt(0) lgkmcnt(0)" ::: "memory"); if (tid == 0) st_flag(ws + WS_SF + s * 4, tag); STAMP_FLUSH(256 + s); return; }
#define SCHED() __builtin_amdgcn_sched_barrier(0)

#define STAMP(i)
#define STAMP_DECL
#define STAMP_FLUSH(b)

constexpr u32 WS_EPOCH = 0;
constexpr u32 WS_SF = 2048;
constexpr u32 WS_G = 4096;
constexpr u32 WS_OF = 16384;
constexpr u32 WS_KVP = 65536;
constexpr u32 WS_XT = 2u << 20;
constexpr u32 WS_O = 6u << 20;
constexpr u32 WS_BT = 8u << 20;
constexpr u32 WS_END = 10u << 20;
constexpr u32 SPIN_CAP = 1u << 16;

DI float u2f(u32 u) { return __builtin_bit_cast(float, u); }
DI u32 f2u(float f) { return __builtin_bit_cast(u32, f); }
DI bool anyl(bool p) { return __builtin_amdgcn_ballot_w64(p) != 0; }
DI float exp_acc(float x) {
  const float t = x * 1.4426950408889634f, r = __builtin_fmaf(x, 1.4426950408889634f, -t) + x * 1.925963033500011e-8f;
  const float e = __builtin_amdgcn_exp2f(t);
  return __builtin_fmaf(e, r * 0.6931471805599453f, e);
}
DI u32 pk2(float a, float b) { f2 v = {a, b}; return __builtin_bit_cast(u32, __builtin_convertvector(v, bf2)); }
DI u32 pk2(f2 v) { return __builtin_bit_cast(u32, __builtin_convertvector(v, bf2)); }
DI float blo(u32 u) { return u2f(u << 16); }
DI float bhi(u32 u) { return u2f(u & 0xffff0000u); }
DI f2 unpk(u32 u) { return f2{blo(u), bhi(u)}; }
DI float rbf(float a) { return blo(pk2(a, a)); }
DI float fmx(float a, float b) { return __builtin_elementwise_maximum(a, b); }
DI float dot2(u32 a, u32 b, float c) {
  return __builtin_amdgcn_fdot2_f32_bf16(__builtin_bit_cast(bf2, a), __builtin_bit_cast(bf2, b), c, false);
}
DI f16v mfma32(v4u a, v4u b, f16v c) {
  return __builtin_amdgcn_mfma_f32_32x32x16_bf16(__builtin_bit_cast(bf8, a), __builtin_bit_cast(bf8, b), c, 0, 0, 0);
}
DI f4 mfma16(v4u a, v4u b, f4 c) {
  return __builtin_amdgcn_mfma_f32_16x16x32_bf16(__builtin_bit_cast(bf8, a), __builtin_bit_cast(bf8, b), c, 0, 0, 0);
}
DI v2u trd(LAS char* p) { return __builtin_bit_cast(v2u, __builtin_amdgcn_ds_read_tr16_b64_v4i16((LAS s16x4*)p)); }
DI v4u trd2(LAS char* p0, LAS char* p1) { v2u a = trd(p0), b = trd(p1); return v4u{a.x, a.y, b.x, b.y}; }
DI v4u ld128(LAS char* p) { return *(LAS v4u*)p; }
DI v4u mksrd(const void* p, u32 bytes) { const u64 a = (u64)p; return v4u{(u32)a, (u32)(a >> 32) & 0xffffu, bytes, 0x00020000u}; }
#define DMA1(W, MOD, srd, voff, soff, lds) \
  asm volatile("s_mov_b32 m0, %0\n\ts_nop 0\n\tbuffer_load_" W " %1, %2, %3 offen " MOD " lds" ::"s"(lds), "v"(voff), "s"(srd), "s"(soff) : "m0", "memory")
#define DMA4(MOD, srd, v0, v1, v2, v3, soff, lds) \
  asm volatile("s_mov_b32 m0, %0\n\ts_nop 0\n\tbuffer_load_dwordx4 %1, %5, %6 offen " MOD " lds\n\t" \
               "buffer_load_dwordx4 %2, %5, %6 offen offset:1024 " MOD " lds\n\t" \
               "buffer_load_dwordx4 %3, %5, %6 offen offset:2048 " MOD " lds\n\t" \
               "buffer_load_dwordx4 %4, %5, %6 offen offset:3072 " MOD " lds" \
               ::"s"(lds), "v"(v0), "v"(v1), "v"(v2), "v"(v3), "s"(srd), "s"(soff) : "m0", "memory")
#define DMA2(MOD, srd, v0, v1, soff, lds) \
  asm volatile("s_mov_b32 m0, %0\n\ts_nop 0\n\tbuffer_load_dwordx4 %1, %3, %4 offen " MOD " lds\n\t" \
               "buffer_load_dwordx4 %2, %3, %4 offen offset:1024 " MOD " lds" \
               ::"s"(lds), "v"(v0), "v"(v1), "s"(srd), "s"(soff) : "m0", "memory")
#define WAITBAR(n) asm volatile("s_waitcnt vmcnt(" #n ") lgkmcnt(0)\n\ts_barrier" ::: "memory")
#define LBAR() asm volatile("s_waitcnt lgkmcnt(0)\n\ts_barrier" ::: "memory")
#define LWAIT() asm volatile("s_waitcnt lgkmcnt(0)" ::: "memory")
DI v4u pack8(const f16v& a, int o) {
  return v4u{pk2(a[o], a[o + 1]), pk2(a[o + 2], a[o + 3]), pk2(a[o + 4], a[o + 5]), pk2(a[o + 6], a[o + 7])};
}
DI __amdgpu_buffer_rsrc_t mkrsrc(const void* p, u32 bytes) {
  return __builtin_amdgcn_make_buffer_rsrc((void*)p, 0, bytes, 0x00020000);
}
DI void st_flag(char* p, u32 v) { __hip_atomic_store((GAS u32*)p, v, __ATOMIC_RELAXED, __HIP_MEMORY_SCOPE_AGENT); }
DI void wait_flags(char* p, u32 tag) {
#pragma unroll 1
  for (u32 spins = 0; spins < SPIN_CAP; ++spins) {
    const u32 v = __hip_atomic_load((GAS u32*)p, __ATOMIC_RELAXED, __HIP_MEMORY_SCOPE_AGENT);
    if (!anyl(v != tag)) break;
    __builtin_amdgcn_s_sleep(1);
  }
  asm volatile("" ::: "memory");
}
DI void swap32(float& a, float& b) {
  auto r = __builtin_amdgcn_permlane32_swap(f2u(a), f2u(b), false, false);
  a = u2f(r[0]); b = u2f(r[1]);
}

#define PIN(x) asm volatile("" : "+v"(x))
struct Smx {
  float cp[2], cn[2]; f2 ep[2][4], en[2][4];
  static constexpr float L = 1.4426950408889634f;
  DI void step(int k, const f16v& a, v4u (&pos)[2], v4u (&neg)[2]) {
    if (k < 2) {
      const int o = 8 * k;
      cp[k] = fmx(fmx(fmx(fmx(a[o], a[o + 1]), a[o + 2]), fmx(fmx(a[o + 3], a[o + 4]), a[o + 5])), fmx(a[o + 6], a[o + 7]));
      cn[k] = fmx(fmx(fmx(fmx(-a[o], -a[o + 1]), -a[o + 2]), fmx(fmx(-a[o + 3], -a[o + 4]), -a[o + 5])), fmx(-a[o + 6], -a[o + 7]));
      PIN(cp[k]); PIN(cn[k]);
    } else if (k == 2) {
      swap32(cp[0], cn[0]); swap32(cp[1], cn[1]);
      cp[0] = fmx(cp[0], cn[0]) * -L; cp[1] = fmx(cp[1], cn[1]) * -L;
      cn[0] = cp[0]; cn[1] = cp[1];
      swap32(cp[0], cn[0]); swap32(cp[1], cn[1]);
      PIN(cp[0]); PIN(cn[0]); PIN(cp[1]); PIN(cn[1]);
    } else if (k < 11) {
      const int d = (k - 3) >> 2, i = (k - 3) & 3;
      const f2 p = {a[8 * d + 2 * i], a[8 * d + 2 * i + 1]};
      const f2 u = __builtin_elementwise_fma(p, f2{L, L}, f2{cp[d], cp[d]});
      const f2 v = __builtin_elementwise_fma(p, f2{-L, -L}, f2{cn[d], cn[d]});
      ep[d][i] = f2{__builtin_amdgcn_exp2f(u.x), __builtin_amdgcn_exp2f(u.y)};
      en[d][i] = f2{__builtin_amdgcn_exp2f(v.x), __builtin_amdgcn_exp2f(v.y)};
      PIN(ep[d][i]); PIN(en[d][i]);
    } else if (k < 13) {
      const int d = k - 11;
      const f2 s2 = (ep[d][0] + ep[d][1]) + (ep[d][2] + ep[d][3]), t2 = (en[d][0] + en[d][1]) + (en[d][2] + en[d][3]);
      cp[d] = s2.x + s2.y; cn[d] = t2.x + t2.y;
      PIN(cp[d]); PIN(cn[d]);
    } else if (k == 13) {
      swap32(cp[0], cn[0]); swap32(cp[1], cn[1]);
      cp[0] = __builtin_amdgcn_rcpf(cp[0] + cn[0]); cp[1] = __builtin_amdgcn_rcpf(cp[1] + cn[1]);
      PIN(cp[0]); PIN(cp[1]);
    } else if (k == 14) {
      cn[0] = cp[0]; cn[1] = cp[1];
      swap32(cp[0], cn[0]); swap32(cp[1], cn[1]);
      PIN(cp[0]); PIN(cn[0]); PIN(cp[1]); PIN(cn[1]);
    } else {
      const int d = (k - 15) >> 1;
      if ((k - 15) & 1) {
        const f2 r = {cn[d], cn[d]};
        neg[d] = v4u{pk2(en[d][0] * r), pk2(en[d][1] * r), pk2(en[d][2] * r), pk2(en[d][3] * r)};
        PIN(neg[d]);
      } else {
        const f2 r = {cp[d], cp[d]};
        pos[d] = v4u{pk2(ep[d][0] * r), pk2(ep[d][1] * r), pk2(ep[d][2] * r), pk2(ep[d][3] * r)};
        PIN(pos[d]);
      }
    }
  }
};
template <int COL, int AT>
DI void dmac(v4u srd, u32 voff, u32 soff, u32 base) {
  asm volatile("s_add_u32 m0, %0, %1\n\ts_nop 0\n\tbuffer_load_dwordx4 %2, %3, %4 offen offset:%5 lds"
               ::"s"(base), "i"(AT - COL), "v"(voff), "s"(srd), "s"(soff), "i"(COL) : "m0", "scc", "memory");
}
DI void dma4(v4u srd, u32 v0, u32 v1, u32 v2, u32 v3, u32 soff, u32 lds) { DMA4("", srd, v0, v1, v2, v3, soff, lds); }
DI u32 la(LAS char* p) { return (u32)(size_t)p; }
DI LAS char* lp(u32 a) { return (LAS char*)(size_t)a; }

template <int N> struct ic { static constexpr int value = N; };
constexpr int SLOT = 20480;
constexpr int G_X = 61440;
constexpr int G_B = 65536, G_G = 73728, G_KV = 16384;
constexpr int wv_at(int c) { return c < 5 ? G_X + 4096 * c : 2 * SLOT + 4096 * (c - 5); }
constexpr int wq_at(int c) { return c < 5 ? 4096 * c : c < 7 ? 53248 + 4096 * (c - 5) : 36864; }
constexpr int wo_at(int g) { return g < 4 ? SLOT + 4096 * g : g < 7 ? 2 * SLOT + 4096 * (g - 4) : g < 12 ? G_X + 4096 * (g - 7) : 4096 * (g - 12); }
#define GROUPS(n, a_, na, b_, nb) \
  _Pragma("unroll") for (int i_ = 0; i_ < n; ++i_) { __builtin_amdgcn_sched_group_barrier(a_, na, 0); __builtin_amdgcn_sched_group_barrier(b_, nb, 0); }
DI void attn_block(int b, const char* x, const char* wqkv, const char* wphi, const char* wo, char* ws, char* out,
                   LAS char* sm) {
  STAMP_DECL; STAMP(0);
  const int tid = TID, lane = tid & 63, w = __builtin_amdgcn_readfirstlane(tid >> 6);
  const int xcd = b & 7, j = b >> 3, a = j & 7, c = j >> 3;
  const int rt = (xcd >> 1) * 4 + c, hg = (xcd & 1) * 8 + a;
  const int rot = ((a & 1) + (c & 1) * 2) * 2;
  const int h = lane >> 5, r32 = lane & 31;
  const int G = lane >> 4, q4 = (lane & 15) >> 2, p4 = lane & 3, blk = G & 1;

  const v4u xd = mksrd(x, 2u << 20), wd = mksrd(wqkv, 1536u * 1024u), pd = mksrd(wphi, 16384u);
  const u32 lb = (u32)(size_t)sm;
  const u32 xb = (rt * 128 + w * 32) * 1024;
  const u32 xo0 = (lane >> 3) * 1024 + (((lane & 7) ^ G) << 4);
  const u32 xo1 = (xo0 ^ 64) + 7168, xo2 = xo0 + 2 * 7168, xo3 = xo1 + 2 * 7168;
  const u32 wb = hg * 64 + 16 * w * 3072;
  const u32 wv = (lane >> 2) * 3072 + (lane & 3) * 16;
  const u32 lw = lb + w * 1024;
  u32 wc8[8];
#pragma unroll
  for (int i = 0; i < 8; ++i) wc8[i] = wb + ((i + rot) & 7) * (64 * 3072);
#define WDMA(s0, col, at) dmac<col, at>(wd, wv, wc8[s0], lw)
#define stage(s0) do { \
    dma4(xd, xo0, xo1, xo2, xo3, xb + (((s0) + rot) & 7) * 128, lb + ((s0) % 3) * SLOT + w * 4096); \
    WDMA(s0, 1024, ((s0) % 3) * SLOT + 16384); } while (0)
  stage(0); stage(1);
  DMA1("dword", "", pd, tid * 4, hg * 1024, lb + G_X + w * 256);
  stage(2);
  PRIO(2);
  SCHED();
  const u32 tag = *(const CAS u32*)(ws + WS_EPOCH) + 1u;

  LAS char* xr[4];
#pragma unroll
  for (int ks = 0; ks < 4; ++ks) xr[ks] = sm + w * 4096 + r32 * 128 + (((2 * ks + h) ^ ((r32 >> 1) & 7)) << 4);
  LAS char* wr = sm + (8 * h + q4) * 64 + blk * 32 + p4 * 8;
  auto wfrag = [&](int at, int ks) { return trd2(wr + at + ks * 1024, wr + at + ks * 1024 + 256); };
  u32 a_row, a_va, a_pb0, a_pb1, a_vw;

  v4u xf[32];
  v4u wp0, wp1;
  f16v ak = {};
  u32 a_hi;
  auto kstep = [&](auto S0) {
    constexpr int s0 = S0.value;
    if (s0 == 0) WAITBAR(11);
    else if (s0 == 7) WAITBAR(2);
    else WAITBAR(5);
    if (s0 == 0) STAMP(1);
    constexpr int so = (s0 % 3) * SLOT;
    v4u fk[4];
#pragma unroll
    for (int ks = 0; ks < 4; ++ks) { fk[ks] = wfrag(so + 16384, ks); xf[s0 * 4 + ks] = ld128(xr[ks] + so); }
    if constexpr (s0 >= 1 && s0 <= 5) stage(s0 + 2);
    if constexpr (s0 == 6) { WDMA(0, 2048, wv_at(0)); WDMA(1, 2048, wv_at(1)); }
    if constexpr (s0 == 7) { WDMA(2, 2048, wv_at(2)); WDMA(3, 2048, wv_at(3)); WDMA(4, 2048, wv_at(4)); WDMA(5, 2048, wv_at(5)); }
    if (s0 == 1) {
      LAS char* pp = sm + G_X + blk * 512 + (4 * h + q4) * 32 + p4 * 8;
      const v4u wph = trd2(pp, pp + 256), zero4 = {0, 0, 0, 0};
      wp0 = blk == 0 ? wph : zero4; wp1 = blk == 0 ? zero4 : wph;
      PIN(wp0); PIN(wp1);
    }
    if (s0 == 2) {
      const int hd = w & 1, fh = w >> 1;
      a_row = la(sm + SLOT + w * 4096 + r32 * 64 + ((h ^ ((r32 >> 1) & 7)) << 3));
      a_va = la(sm + G_X + (G >> 1) * 1024 + (blk * 32 + hd * 16 + (lane & 15)) * 16);
      a_vw = la(sm + G_X + w * 2048 + lane * 16);
      a_hi = la(wr + 40960);
      const int t0 = 16 * (G >> 1) + 4 * blk + q4;
      a_pb0 = la(sm + SLOT + hd * 2048 + t0 * 64 + (((4 * fh + p4) ^ ((t0 >> 1) & 7)) << 3));
      a_pb1 = la(sm + SLOT + hd * 2048 + (t0 + 8) * 64 + (((4 * fh + p4) ^ (((t0 + 8) >> 1) & 7)) << 3));
      PIN(a_row); PIN(a_va); PIN(a_pb0); PIN(a_pb1); PIN(a_vw); PIN(a_hi);
    }
    SCHED();
#pragma unroll
    for (int ks = 0; ks < 4; ++ks) ak = mfma32(fk[ks], xf[s0 * 4 + ks], ak);
    PIN(ak);
    SCHED();
  };
  kstep(ic<0>{}); kstep(ic<1>{}); kstep(ic<2>{}); kstep(ic<3>{}); kstep(ic<4>{}); kstep(ic<5>{}); kstep(ic<6>{}); kstep(ic<7>{});
  STAMP(2);
  {
    f16v pk = {}, av = {};
    pk = mfma32(wp0, pack8(ak, 0), pk);
    pk = mfma32(wp1, pack8(ak, 8), pk);
    PIN(pk);
    v4u fv[4];
    auto pre = [&](int n) {
      LAS char* p = lp(a_hi) + (wv_at(n >> 2) - 40960 + (n & 3) * 1024);
      fv[n & 3] = trd2(p, p + 256);
    };
    auto vm = [&](int n) { av = mfma32(xf[n], fv[n & 3], av); PIN(av); };
    Smx m;
    v4u pos[2], neg[2];
    WAITBAR(4);
    STAMP(12);
    pre(0); pre(1); pre(2); pre(3);
    WDMA(6, 2048, wv_at(6)); WDMA(7, 2048, wv_at(7));
    SCHED();
    m.step(0, pk, pos, neg); m.step(1, pk, pos, neg);
    SCHED();
#pragma unroll
    for (int n = 0; n < 8; ++n) {
      vm(n);
      if (n < 4) pre(n + 4);
      SCHED();
      m.step(2 + n, pk, pos, neg);
      SCHED();
    }
    WAITBAR(2);
    STAMP(13);
    pre(8); pre(9); pre(10); pre(11);
    WDMA(0, 0, wq_at(0)); WDMA(1, 0, wq_at(1)); WDMA(2, 0, wq_at(2));
    SCHED();
    m.step(10, pk, pos, neg);
    SCHED();
#pragma unroll
    for (int n = 8; n < 24; ++n) {
      vm(n);
      if (n < 20) pre(n + 4);
      SCHED();
      if (!(n & 1)) m.step(11 + ((n - 8) >> 1), pk, pos, neg);
      if (n == 19) { *(LAS v2u*)lp(a_row) = v2u{pos[0].x, pos[0].y}; *(LAS v2u*)lp(a_row ^ 16) = v2u{pos[0].z, pos[0].w}; }
      if (n == 21) { *(LAS v2u*)lp(a_row ^ 32) = v2u{neg[0].x, neg[0].y}; *(LAS v2u*)lp(a_row ^ 48) = v2u{neg[0].z, neg[0].w}; }
      if (n == 23) { *(LAS v2u*)(lp(a_row) + 2048) = v2u{pos[1].x, pos[1].y}; *(LAS v2u*)(lp(a_row ^ 16) + 2048) = v2u{pos[1].z, pos[1].w}; }
      SCHED();
    }
    WAITBAR(3);
    STAMP(14);
    pre(24); pre(25); pre(26); pre(27);
    *(LAS v2u*)(lp(a_row ^ 32) + 2048) = v2u{neg[1].x, neg[1].y}; *(LAS v2u*)(lp(a_row ^ 48) + 2048) = v2u{neg[1].z, neg[1].w};
    WDMA(3, 0, wq_at(3)); WDMA(4, 0, wq_at(4));
    SCHED();
#pragma unroll
    for (int n = 24; n < 32; ++n) {
      vm(n);
      if (n < 28) pre(n + 4);
      if (n == 28) WDMA(5, 0, wq_at(5));
      if (n == 29) WDMA(6, 0, wq_at(6));
      if (n == 30) WDMA(7, 0, wq_at(7));
      SCHED();
    }
    *(LAS v4u*)lp(a_vw) = pack8(av, 0);
    *(LAS v4u*)(lp(a_vw) + 1024) = pack8(av, 8);
  }
  LBAR();
  STAMP(15);
  const __amdgpu_buffer_rsrc_t kvr = mkrsrc(ws + WS_KVP + hg * 65536, 65536);
  {
    v4u fa[4], fb[4];
#pragma unroll
    for (int t = 0; t < 4; ++t) { fb[t] = trd2(lp(a_pb0) + t * 4096, lp(a_pb1) + t * 4096); fa[t] = ld128(lp(a_va) + t * 2048); }
    SCHED();
    const v4u ones = {0x3f803f80u, 0x3f803f80u, 0x3f803f80u, 0x3f803f80u};
    f4 kv = {}, z = {};
#pragma unroll
    for (int t = 0; t < 4; ++t) { z = mfma16(ones, fb[t], z); kv = mfma16(fa[t], fb[t], kv); }
    const u32 zt = __builtin_amdgcn_perm(pk2(z[0], z[0]), tag, 0x07060100u);
    __builtin_amdgcn_raw_buffer_store_b128(v4u{pk2(kv[0], kv[1]), tag, pk2(kv[2], kv[3]), zt}, kvr, tid * 16, rt * 4096, 16);
  }
  STAMP(3);
  WAITBAR(1);
  STAMP(16);
  const int tb = rt * 2 + (hg & 1), cb = hg >> 1;
  const v4u od = mksrd(wo, 512u * 1024u);
  const u32 so0 = (lane >> 3) * 1024 + (((lane & 7) ^ (blk << 2)) << 4);
  const u32 ob0 = cb * 128 + w * 8192;
#define ODMA(g) dmac<0, wo_at(g)>(od, so0, ob0 + (g) * 32768, lw)
  f16v aq = {};
  {
    v4u fq[8];
    auto pre = [&](int n) {
      const int at = wq_at(n >> 2) + (n & 3) * 1024;
      fq[n & 7] = trd2(wr + at, wr + at + 256);
    };
#pragma unroll
    for (int n = 0; n < 6; ++n) pre(n);
    auto qs = [&](auto N) {
      constexpr int n = N.value;
      SCHED();
      aq = mfma32(fq[n & 7], xf[n], aq); PIN(aq);
      aq = mfma32(fq[(n + 1) & 7], xf[n + 1], aq); PIN(aq);
      if constexpr (n < 26) { pre(n + 6); pre(n + 7 < 32 ? n + 7 : 31); }
      if constexpr (n < 24) ODMA(n >> 1);
    };
    qs(ic<0>{}); qs(ic<2>{}); qs(ic<4>{}); qs(ic<6>{}); qs(ic<8>{}); qs(ic<10>{}); qs(ic<12>{}); qs(ic<14>{});
    qs(ic<16>{}); qs(ic<18>{}); qs(ic<20>{}); qs(ic<22>{}); qs(ic<24>{}); qs(ic<26>{}); qs(ic<28>{}); qs(ic<30>{});
    SCHED();
  }
  STAMP(4);
  v4u rec[16];
  const u32 ro = tid * 16;
  v4u qp[2], qn[2];
  {
#pragma unroll
    for (int p = 0; p < 4; ++p) rec[p] = __builtin_amdgcn_raw_buffer_load_b128(kvr, ro, p * 4096, 16);
    SCHED();
    f16v pq = {};
    pq = mfma32(wp0, pack8(aq, 0), pq);
    pq = mfma32(wp1, pack8(aq, 8), pq);
    PIN(pq);
    LBAR();
    Smx m;
#pragma unroll
    for (int k = 0; k < 19; ++k) {
      if (k < 12) rec[4 + k] = __builtin_amdgcn_raw_buffer_load_b128(kvr, ro, (4 + k) * 4096, 16);
      SCHED();
      m.step(k, pq, qp, qn);
      SCHED();
    }
  }
  ODMA(12); ODMA(13); ODMA(14); ODMA(15);
  STAMP(5);
  float s0 = 0.f, s1 = 0.f, s2 = 0.f, s3 = 0.f, sz = 0.f;
  {
    u32 b1 = 0, b2 = 0;
#pragma unroll
    for (int p = 0; p < 16; ++p) {
      b1 = __builtin_amdgcn_bitop3_b32(b1, rec[p].y, tag, 0xf6);
      b2 = __builtin_amdgcn_bitop3_b32(b2, rec[p].w, tag, 0xf6);
      s0 += blo(rec[p].x); s1 += bhi(rec[p].x); s2 += blo(rec[p].z); s3 += bhi(rec[p].z);
      sz += bhi(rec[p].w);
    }
    if (anyl((b1 | (b2 & 0xffffu)) != 0)) {
#pragma unroll 1
      for (u32 spins = 0; spins < SPIN_CAP; ++spins) {
        bool all = true;
#pragma unroll
        for (int p = 0; p < 16; ++p) {
          if (anyl(__builtin_amdgcn_bitop3_b32(rec[p].y ^ tag, rec[p].w ^ tag, 0xffffu, 0xf8) != 0)) {
            rec[p] = __builtin_amdgcn_raw_buffer_load_b128(kvr, ro, p * 4096, 16);
            all = false;
          }
        }
        if (all) break;
        asm volatile("" ::: "memory");
      }
      s0 = s1 = s2 = s3 = sz = 0.f;
#pragma unroll
      for (int p = 0; p < 16; ++p) {
        s0 += blo(rec[p].x); s1 += bhi(rec[p].x); s2 += blo(rec[p].z); s3 += bhi(rec[p].z);
        sz += bhi(rec[p].w);
      }
    }
  }
  STAMP(6);
  {
    LAS char* p = sm + G_KV + (w & 1) * 2048 + ((w >> 1) * 16 + (lane & 15)) * 64 + G * 8;
    const u32 zz = pk2(sz, sz);
    *(LAS v2u*)p = v2u{pk2(s0, s1), pk2(s2, s3)};
    *(LAS v2u*)(p + 32) = v2u{zz, zz};
  }
  LBAR();
  const __amdgpu_buffer_rsrc_t wsr = mkrsrc(ws, WS_END);
  {
    LAS char* im = sm + G_KV + (4 * h + q4) * 64 + blk * 32 + p4 * 8;
    v4u f[4];
#pragma unroll
    for (int i = 0; i < 4; ++i) f[i] = trd2(im + i * 1024, im + i * 1024 + 512);
    f16v n0 = {}, n1 = {};
    n0 = mfma32(f[0], qp[0], n0); n1 = mfma32(f[2], qp[1], n1);
    n0 = mfma32(f[1], qn[0], n0); n1 = mfma32(f[3], qn[1], n1);
    const float i0 = __builtin_amdgcn_rcpf(n0[8] + 1e-6f), i1 = __builtin_amdgcn_rcpf(n1[8] + 1e-6f);
    const f2 j0 = {i0, i0}, j1 = {i1, i1};
    const u32 oo = WS_O + ((rt * 4 + w) * 32 + hg * 2) * 1024;
    __builtin_amdgcn_raw_buffer_store_b128(v4u{pk2(f2{n0[0], n0[1]} * j0), pk2(f2{n0[2], n0[3]} * j0), pk2(f2{n0[4], n0[5]} * j0), pk2(f2{n0[6], n0[7]} * j0)},
                                           wsr, lane * 16, oo, 16);
    __builtin_amdgcn_raw_buffer_store_b128(v4u{pk2(f2{n1[0], n1[1]} * j1), pk2(f2{n1[2], n1[3]} * j1), pk2(f2{n1[4], n1[5]} * j1), pk2(f2{n1[6], n1[7]} * j1)},
                                           wsr, lane * 16 + 1024, oo, 16);
  }
  STAMP(7);
  WAITBAR(0);
  if (tid == 0) st_flag(ws + WS_OF + (rt * 16 + hg) * 128, tag);
  STAMP(8);
  const int wc = w & 1, wt = w >> 1;
  v4u wf[32];
  {
    LAS char* wr2 = sm + (4 * h + q4) * 128 + (((4 * wc + 2 * blk + (p4 >> 1)) ^ ((q4 >> 1) << 2)) << 4) + (p4 & 1) * 8;
#pragma unroll
    for (int i = 0; i < 32; ++i) wf[i] = trd2(wr2 + wo_at(i >> 1) + (i & 1) * 2048, wr2 + wo_at(i >> 1) + (i & 1) * 2048 + 1024);
  }
  LBAR();
  STAMP(17);
  {
    const u32 fo = lane < 16 || w != 0 ? WS_OF + (rt * 16 + (lane & 15)) * 128
                 : WS_SF + 4 * (lane < 48 ? cb * 32 + lane - 16 : (lane < 56 || b != 0) ? tb * 8 + (lane & 7) : (lane & 7) * 32);
    wait_flags(ws + fo, tag);
  }
  STAMP(9);
  const v4u sd = mksrd(ws, WS_END);
  {
    const u32 lo = lane * 16;
    const u32 ob = WS_O + ((tb * 2 + wt) * 32 + wc * 4) * 1024;
#pragma unroll
    for (int s = 0; s < 4; ++s) DMA4("sc1", sd, lo, lo, lo, lo, ob + s * 8192, lb + s * 16384 + w * 4096);
    if (w == 0) {
      const u32 bo = (lane >> 3) * 4096 + (((lane & 7) ^ (blk << 2)) << 4);
      const u32 b1 = bo + 31744, b2 = bo + 2 * 31744, b3 = bo + 3 * 31744;
      DMA4("sc1", sd, bo, b1, b2, b3, WS_BT + cb * 64 * 4096 + tb * 128, lb + G_B);
      DMA4("sc1", sd, bo, b1, b2, b3, WS_BT + (cb * 64 + 32) * 4096 + tb * 128, lb + G_B + 4096);
      DMA1("dword", "sc1", sd, lane * 4, WS_G + tb * 256, lb + G_G);
    }
  }
  if (b == 0 && tid == 0) st_flag(ws + WS_EPOCH, tag);
  f16v acc = {};
  {
    LAS char* orr = sm + wt * 8192 + lane * 16;
#pragma unroll
    for (int s = 0; s < 4; ++s) {
      if (s == 0) { if (w == 0) WAITBAR(21); else WAITBAR(12); }
      else if (s == 1) { if (w == 0) WAITBAR(17); else WAITBAR(8); }
      else if (s == 2) { if (w == 0) WAITBAR(13); else WAITBAR(4); }
      else WAITBAR(0);
      if (s == 0) STAMP(18);
      if (s == 3) STAMP(19);
      v4u fo[8];
#pragma unroll
      for (int i = 0; i < 8; ++i) fo[i] = ld128(orr + s * 16384 + i * 1024);
      SCHED();
#pragma unroll
      for (int i = 0; i < 8; ++i) acc = mfma32(wf[s * 8 + i], fo[i], acc);
    }
  }
  STAMP(10);
  {
    const int t = wt * 32 + r32;
    const float g = *(LAS float*)(sm + G_G + t * 4), g1 = rbf(1.f - g);
    LAS char* br = sm + G_B + (32 * wc + 4 * h + q4) * 128 + (((4 * wt + 2 * blk + (p4 >> 1)) ^ ((q4 >> 1) << 2)) << 4) + (p4 & 1) * 8;
    const f2 gg = {g, g}, gh = {g1, g1};
    v2u o[4];
#pragma unroll
    for (int k = 0; k < 4; ++k) {
      const v2u bv = trd(br + k * 1024);
      o[k] = v2u{pk2(__builtin_elementwise_fma(f2{acc[4 * k], acc[4 * k + 1]}, gg, gh * unpk(bv.x))),
                 pk2(__builtin_elementwise_fma(f2{acc[4 * k + 2], acc[4 * k + 3]}, gg, gh * unpk(bv.y)))};
    }
    char* op = out + (size_t)(tb * 64 + t) * 1024 + cb * 128 + wc * 64 + h * 16;
#pragma unroll
    for (int k = 0; k < 4; k += 2) {
      auto rx = __builtin_amdgcn_permlane32_swap(o[k].x, o[k + 1].x, false, false);
      auto ry = __builtin_amdgcn_permlane32_swap(o[k].y, o[k + 1].y, false, false);
      *(GAS v4u*)(op + k * 16) = v4u{rx[0], ry[0], rx[1], ry[1]};
    }
  }
  STAMP(11);
  STAMP_FLUSH(b);
}

DI f2 cmul(f2 a, f2 b) { return f2{__builtin_fmaf(a.x, b.x, -(a.y * b.y)), __builtin_fmaf(a.x, b.y, a.y * b.x)}; }
DI u32 pkc(f2 a) { return pk2(a.x, a.y); }
DI u32 pkn(f2 a) { return pk2(a.x, -a.y); }
DI f2 cexp2(float lr, float li, float t) {
  const float e = __builtin_amdgcn_exp2f(lr * t), ph = __builtin_amdgcn_fractf(li * t);
  return f2{e, e} * f2{__builtin_amdgcn_cosf(ph), __builtin_amdgcn_sinf(ph)};
}
constexpr u32 kc_index(int lag) {
  if (lag < 0) return 768;
  const int sh = lag < 256 ? 0 : lag < 512 ? 1 : lag < 1024 ? 2 : 3;
  const int q = 1 << sh, m = lag >> sh, rem = lag & (q - 1);
  const int v = (m + ((2 * rem > q || (2 * rem == q && (m & 1))) ? 1 : 0)) << sh;
  const int col = v & 0xf0;
  return v < 256    ? v
         : v < 512  ? 256 + col + ((v & 2) ? 12 : 0) + ((v >> 2) & 3)
         : v < 1024 ? 256 + col + 4 + ((v >> 6) & 4) + ((v >> 2) & 3)
         : v < 2048 ? 512 + col + ((v & 8) ? 12 : 0) + ((v >> 8) & 3)
                    : 516;
}
struct KcTab { u32 v[1024]; };
constexpr KcTab make_kc() {
  KcTab t = {};
  for (int i = 0; i < 256; ++i)
    for (int e = 0; e < 9; ++e) t.v[4 * i + e / 3] |= kc_index(2047 - 8 * i - e) << (1 + 10 * (e % 3));
  return t;
}
__device__ __attribute__((aligned(16))) constexpr KcTab kc_tab = make_kc();

constexpr int E_IMG = 8704;
constexpr int S_TBL = 0, TBL_CH = 8448, TBL_ODD = 4160;
constexpr int S_RED = 20480;
constexpr int S_U = 69632, U_CH = 4576;
constexpr int S_KC = 78784, KC_CH = 1552;

DI void ssm_block(int s, const char* x, const char* Are, const char* Aim, const char* Bre, const char* Bim,
                  const char* Cre, const char* Cim, const char* logdt, const char* dsk, const char* gw, char* ws,
                  LAS char* sm) {
  STAMP_DECL; STAMP(0);
  const int tid = TID, lane = tid & 63, w = __builtin_amdgcn_readfirstlane(tid >> 6);
  const int grp = s >> 5, ta = s & 31;
  const int G = lane >> 4, q4 = (lane & 15) >> 2, p4 = lane & 3, ci = lane & 15;
  const int ch = w >> 1;
  const u32 lb = (u32)(size_t)sm;
  PRIO(3);
  {
    const v4u xd = mksrd(x, 2u << 20);
    const u32 xo = (lane >> 3) * 1024 + (((lane & 7) ^ (2 * G)) << 4);
    DMA2("", xd, xo, xo + 7168, (ta * 64 + w * 16) * 1024 + grp * 128, lb + w * 2048);
  }
  {
    const char* pa[6] = {Are, Aim, Bre, Bim, Cre, Cim};
#pragma unroll
    for (int i = 0; i < 3; ++i) {
      const v4u d = mksrd(w < 2 ? pa[2 * i] : pa[2 * i + 1], 131072u);
      DMA1("dword", "", d, lane * 4, s * 512 + (w & 1) * 256, lb + 16384 + i * 1024 + w * 256);
    }
  }
  const u32 ldt2 = *(const CAS u32*)(logdt + s * 4), dsk2 = *(const CAS u32*)(dsk + s * 4);
  SCHED();
  const u32 tag = *(const CAS u32*)(ws + WS_EPOCH) + 1u;
  const __amdgpu_buffer_rsrc_t wsr = mkrsrc(ws, WS_END);
  {
    if (tid < 2) *(LAS u32*)(sm + S_KC + tid * KC_CH + 1536) = 0;
    if (tid >= 64 && tid < 124) {
      const int i = tid - 64;
      *(LAS v4u*)(sm + S_U + (i >= 30 ? U_CH + (i - 30) * 16 : i * 16)) = v4u{0, 0, 0, 0};
    }
    WAITBAR(0);
    STAMP(1);
    SX(1);
    LAS char* im2 = sm + 8192 + w * 2048;
    LAS char* rp = sm + (16 * G + q4) * 128 + (p4 & 1) * 8;
    v2u v[4];
#pragma unroll
    for (int k = 0; k < 4; ++k) v[k] = trd(rp + k * 512 + (((2 * w + (p4 >> 1)) ^ (2 * ((2 * k + (q4 >> 1)) & 3))) << 4));
    *(LAS v4u*)(im2 + ci * 128 + (((2 * G) ^ (ci & 7)) << 4)) = v4u{v[0].x, v[0].y, v[1].x, v[1].y};
    *(LAS v4u*)(im2 + ci * 128 + (((2 * G + 1) ^ (ci & 7)) << 4)) = v4u{v[2].x, v[2].y, v[3].x, v[3].y};
    LWAIT();
    const u32 vo = G * 8192 + ci * 16, so = WS_XT + (grp * 64 + w * 16) * 8192 + ta * 256;
#pragma unroll
    for (int k = 0; k < 4; ++k) {
      const int c = 4 * k + G;
      v[k] = *(LAS v2u*)(im2 + c * 128 + (((ci >> 1) ^ (c & 7)) << 4) + (ci & 1) * 8);
    }
#pragma unroll
    for (int k = 0; k < 4; ++k)
      __builtin_amdgcn_raw_buffer_store_b128(v4u{v[k].x, tag, v[k].y, tag}, wsr, vo, so + k * 32768, 16);
  }
  PRIO(0);
  LAS u16* pp = (LAS u16*)(sm + 16384) + tid;
  const u32 are = pp[0], aim = pp[256], bre = pp[512], bim = pp[768], cre = pp[1024], cim = pp[1280];
  LBAR();
  STAMP(2);
  SX(2);
  {
    const float dt = rbf(exp_acc(ch ? bhi(ldt2) : blo(ldt2)));
    const float lr = (dt * blo(are)) * 1.4426950408889634f, li = (dt * blo(aim)) * 0.15915494309189535f;
    const f2 W = cmul(f2{blo(cre), blo(cim)}, f2{dt * blo(bre), dt * blo(bim)});
    LAS u32* eb = (LAS u32*)(sm + ch * 4 * E_IMG + ((tid & 127) >> 2) * 272 + (tid & 3) * 4);
    constexpr int T = E_IMG / 4;
    f2 e[16];
    e[1] = cexp2(lr, li, 1.f);
    const f2 f1 = cexp2(lr, li, 16.f), c1 = cexp2(lr, li, 256.f);
    e[2] = cmul(e[1], e[1]);
    const f2 f2_ = cmul(f1, f1), c2 = cmul(c1, c1);
    e[3] = cmul(e[2], e[1]); e[4] = cmul(e[2], e[2]);
    const f2 f4_ = cmul(f2_, f2_), c3 = cmul(c2, c1), c4 = cmul(c2, c2);
    f2 a[16];
    a[0] = W; a[1] = cmul(W, f1); a[2] = cmul(W, f2_);
    e[5] = cmul(e[4], e[1]); e[6] = cmul(e[4], e[2]); e[7] = cmul(e[4], e[3]); e[8] = cmul(e[4], e[4]);
    a[3] = cmul(a[1], f2_);
    const f2 f8_ = cmul(f4_, f4_);
    eb[T + 0] = 0x00003f80u;
#pragma unroll
    for (int i = 1; i < 8; ++i) { e[8 + i] = cmul(e[8], e[i]); eb[T + 4 * i] = pkc(e[i]); }
#pragma unroll
    for (int i = 0; i < 4; ++i) { a[4 + i] = cmul(a[i], f4_); eb[4 * i] = pkn(a[i]); }
#pragma unroll
    for (int i = 8; i < 16; ++i) eb[T + 4 * i] = pkc(e[i]);
#pragma unroll
    for (int i = 0; i < 8; ++i) { a[8 + i] = cmul(a[i], f8_); if (i >= 4) eb[4 * i] = pkn(a[i]); }
    const f2 cp = cmul(c1, e[2]);
#pragma unroll
    for (int i = 8; i < 16; ++i) eb[4 * i] = pkn(a[i]);
    const f2 gb[4] = {c1, c2, c3, cp};
#pragma unroll
    for (int g = 0; g < 4; ++g) {
      eb[2 * T + 16 * g] = pkc(gb[g]);
      eb[2 * T + 16 * g + 4] = pkc(cmul(gb[g], e[4]));
      eb[2 * T + 16 * g + 8] = pkc(cmul(gb[g], e[8]));
      eb[2 * T + 16 * g + 12] = pkc(cmul(gb[g], e[12]));
    }
    const f2 d[4] = {c4, cmul(c4, c1), cmul(c4, c2), cmul(c4, c3)};
#pragma unroll
    for (int r = 0; r < 4; ++r) { eb[3 * T + 4 * r] = pkc(d[r]); eb[3 * T + 48 + 4 * r] = pkc(cmul(d[r], e[8])); }
    eb[3 * T + 16] = pkc(cmul(c4, c4));
  }
  const v4u kt = *(const GAS v4u*)((const char*)kc_tab.v + tid * 16);
  LBAR();
  STAMP(3);
  SX(3);
  {
    const int c2 = w & 1, tl = w >> 1;
    LAS char* eb = sm + c2 * 4 * E_IMG + G * 272 + ci * 16;
    LAS char* et = eb + (1 + tl) * E_IMG;
    LAS char* kc = sm + S_KC + c2 * KC_CH + ci * 32 + G * 8;
    v4u fa[8], fb[8];
#pragma unroll
    for (int ks = 0; ks < 8; ++ks) { fa[ks] = ld128(eb + ks * 1088); fb[ks] = ld128(et + ks * 1088); }
    SCHED();
    f4 k0 = {}, k1 = {};
#pragma unroll
    for (int ks = 0; ks < 4; ++ks) { k0 = mfma16(fb[ks], fa[ks], k0); k1 = mfma16(fb[4 + ks], fa[4 + ks], k1); }
    if (tl == 0) {
#pragma unroll
      for (int ks = 0; ks < 8; ++ks) fb[ks] = ld128(eb + 3 * E_IMG + ks * 1088);
      SCHED();
      f4 k2 = {}, k3 = {};
#pragma unroll
      for (int ks = 0; ks < 4; ++ks) { k2 = mfma16(fb[ks], fa[ks], k2); k3 = mfma16(fb[4 + ks], fa[4 + ks], k3); }
      k2 += k3;
      *(LAS v2u*)(kc + 1024) = v2u{pk2(k2[0], k2[1]), pk2(k2[2], k2[3])};
    }
    k0 += k1;
    *(LAS v2u*)(kc + tl * 512) = v2u{pk2(k0[0], k0[1]), pk2(k0[2], k0[3])};
  }
  v4u ur[4];
#pragma unroll
  for (int k = 0; k < 4; ++k) ur[k] = __builtin_amdgcn_raw_buffer_load_b128(wsr, tid * 16, WS_XT + s * 16384 + k * 4096, 16);
  const char* gx = x + (size_t)(s * 8 + w * 2) * 1024 + lane * 16;
  const v4u g0 = *(const GAS v4u*)gx, g1 = *(const GAS v4u*)(gx + 1024), gwv = *(const GAS v4u*)(gw + lane * 16);
  LBAR();
  STAMP(4);
  {
    const u32 ki[9] = {kt.x & 0x7feu, (kt.x >> 10) & 0x7feu, (kt.x >> 20) & 0x7feu, kt.y & 0x7feu, (kt.y >> 10) & 0x7feu, (kt.y >> 20) & 0x7feu,
                       kt.z & 0x7feu, (kt.z >> 10) & 0x7feu, (kt.z >> 20) & 0x7feu};
    u32 kv[2][9];
#pragma unroll
    for (int c2 = 0; c2 < 2; ++c2)
#pragma unroll
      for (int e = 0; e < 9; ++e) kv[c2][e] = *(LAS u16*)(sm + S_KC + c2 * KC_CH + ki[e]);
    SCHED();
#pragma unroll
    for (int c2 = 0; c2 < 2; ++c2) {
      LAS char* tb = sm + S_TBL + c2 * TBL_CH + tid * 16;
      const u32* k = kv[c2];
      *(LAS v4u*)tb = v4u{k[0] | (k[1] << 16), k[2] | (k[3] << 16), k[4] | (k[5] << 16), k[6] | (k[7] << 16)};
      *(LAS v4u*)(tb + TBL_ODD) = v4u{k[1] | (k[2] << 16), k[3] | (k[4] << 16), k[5] | (k[6] << 16), k[7] | (k[8] << 16)};
    }
    if (tid < 8) *(LAS v4u*)(sm + S_TBL + (tid >> 2) * TBL_CH + ((tid >> 1) & 1) * TBL_ODD + 4096 + (tid & 1) * 16) = v4u{0, 0, 0, 0};
  }
#pragma unroll 1
  for (u32 spins = 0; spins < SPIN_CAP; ++spins) {
    bool all = true;
#pragma unroll
    for (int k = 0; k < 4; ++k) {
      if (anyl(((ur[k].y ^ tag) | (ur[k].w ^ tag)) != 0)) {
        ur[k] = __builtin_amdgcn_raw_buffer_load_b128(wsr, tid * 16, WS_XT + s * 16384 + k * 4096, 16);
        all = false;
      }
    }
    if (all) break;
    __builtin_amdgcn_s_sleep(2);
    asm volatile("" ::: "memory");
  }
#pragma unroll
  for (int k = 0; k < 4; ++k)
    *(LAS v2u*)(sm + S_U + (k >> 1) * U_CH + 480 + ((k & 1) * 256 + tid) * 8) = v2u{ur[k].x, ur[k].z};
  {
    float a0 = 0, a1 = 0;
#pragma unroll
    for (int i = 0; i < 4; ++i) { a0 = dot2(g0[i], gwv[i], a0); a1 = dot2(g1[i], gwv[i], a1); }
    swap32(a0, a1);
    float v = a0 + a1;
    v += u2f(__builtin_amdgcn_mov_dpp(f2u(v), 0xB1, 0xf, 0xf, true));
    v += u2f(__builtin_amdgcn_mov_dpp(f2u(v), 0x4E, 0xf, 0xf, true));
    v += u2f(__builtin_amdgcn_mov_dpp(f2u(v), 0x141, 0xf, 0xf, true));
    v += u2f(__builtin_amdgcn_mov_dpp(f2u(v), 0x140, 0xf, 0xf, true));
    {
      auto r = __builtin_amdgcn_permlane16_swap(f2u(v), f2u(v), false, false);
      v = u2f(r[0]) + u2f(r[1]);
    }
    v = rbf(__builtin_amdgcn_rcpf(1.f + __builtin_amdgcn_exp2f(-1.4426950408889634f * rbf(v))));
    if ((lane & 31) == 0) __builtin_amdgcn_raw_buffer_store_b32(f2u(v), wsr, (lane >> 5) * 4, WS_G + (s * 8 + w * 2) * 4, 16);
  }
  LBAR();
  STAMP(5);
  SX(5);
  const int rh = w & 1;
  f4 acc[8] = {};
  {
    LAS char* ub = sm + S_U + ch * U_CH + ci * 32 + G * 16 + (4 - 4 * rh) * 64;
    const int s0 = 2031 + 8 * G - ci;
    u32 kb[4];
#pragma unroll
    for (int i = 0; i < 4; ++i) {
      kb[i] = lb + S_TBL + ch * TBL_CH + ((s0 & 1) ? TBL_ODD : 0) + (s0 >> 1) * 4 - rh * 256 - 59 * 64 + i * 1024;
      asm volatile("" : "+v"(kb[i]));
    }
    v4u bf[2][8], af[2][8];
    auto ld = [&](int rr) {
#pragma unroll
      for (int J = 0; J < 8; ++J) {
        bf[rr & 1][J] = ld128(ub + J * 512 + (3 - rr) * 64);
        LAS u32* kp = (LAS u32*)(size_t)kb[(7 - J) >> 1] + ((7 - J) & 1) * 128 + (3 - rr) * 16;
        af[rr & 1][J] = v4u{kp[0], kp[1], kp[2], kp[3]};
      }
    };
    ld(0);
#pragma unroll
    for (int rr = 0; rr < 4; ++rr) {
      SCHED();
      if (rr < 3) ld(rr + 1);
#pragma unroll
      for (int J = 0; J < 8; ++J)
#pragma unroll
        for (int i = J; i < 8; ++i) acc[i] = mfma16(af[rr & 1][J], bf[rr & 1][i - J], acc[i]);
      if (rr < 3) {
#pragma unroll
        for (int i = 0; i < 24; ++i) {
          __builtin_amdgcn_sched_group_barrier(0x8, 1, 0);
          __builtin_amdgcn_sched_group_barrier(0x100, 1, 0);
        }
        __builtin_amdgcn_sched_group_barrier(0x8, 12, 0);
      }
      SCHED();
    }
  }
  STAMP(6);
  {
    LAS char* mine = sm + S_RED + w * 8192 + lane * 16;
#pragma unroll
    for (int i = 0; i < 8; ++i) *(LAS f4*)(mine + i * 1024) = acc[i];
    LBAR();
    const float dk = ch ? bhi(dsk2) : blo(dsk2);
    const f2 dd = {dk, dk};
    LAS char* pa = sm + S_RED + ch * 16384 + (4 * rh + (lane >> 5)) * 1024 + (lane & 1) * 512 + ((lane >> 1) & 15) * 16;
    LAS char* up = sm + S_U + ch * U_CH + 480 + rh * 2048 + lane * 16;
    const u32 so = WS_BT + (2 * s + ch) * 4096 + rh * 2048;
#pragma unroll
    for (int i = 0; i < 2; ++i) {
      const f4 y0 = *(LAS f4*)(pa + i * 2048) + *(LAS f4*)(pa + i * 2048 + 8192);
      const f4 y1 = *(LAS f4*)(pa + i * 2048 + 256) + *(LAS f4*)(pa + i * 2048 + 8192 + 256);
      const v4u u = ld128(up + i * 1024);
      const v4u r = {pk2(__builtin_elementwise_fma(unpk(u.x), dd, f2{y0[0], y0[1]})), pk2(__builtin_elementwise_fma(unpk(u.y), dd, f2{y0[2], y0[3]})),
                     pk2(__builtin_elementwise_fma(unpk(u.z), dd, f2{y1[0], y1[1]})), pk2(__builtin_elementwise_fma(unpk(u.w), dd, f2{y1[2], y1[3]}))};
      __builtin_amdgcn_raw_buffer_store_b128(r, wsr, lane * 16, so + i * 1024, 16);
    }
  }
  STAMP(7);
  WAITBAR(0);
  if (tid == 0) st_flag(ws + WS_SF + s * 4, tag);
  STAMP(8);
  STAMP_FLUSH(256 + s);
}

__global__ __launch_bounds__(256, 2) void k_all(const char* x, const char* wqkv, const char* wphi, const char* wo,
                                                 const char* Are, const char* Aim, const char* Bre, const char* Bim,
                                                 const char* Cre, const char* Cim, const char* logdt, const char* dsk,
                                                 const char* gw, char* ws, char* out) {
  extern __shared__ __attribute__((aligned(16))) char smem[];
  LAS char* sm = (LAS char*)smem;
  if (BID < 256) attn_block(BID, x, wqkv, wphi, wo, ws, out, sm);
  else ssm_block(BID - 256, x, Are, Aim, Bre, Bim, Cre, Cim, logdt, dsk, gw, ws, sm);
}

NOPK_POP
extern "C" void kernel_launch(void* const* d_in, const int* in_sizes, int n_in, void* d_out, int out_size, void* d_ws,
                              size_t ws_size, hipStream_t stream) {
  (void)in_sizes; (void)n_in; (void)out_size; (void)ws_size;
  (void)hipFuncSetAttribute((const void*)k_all, hipFuncAttributeMaxDynamicSharedMemorySize, 81920);
  const char* const* in = (const char* const*)d_in;
  k_all<<<512, 256, 81920, stream>>>(in[0], in[1], in[2], in[3], in[5], in[6], in[7], in[8], in[9], in[10], in[11],
                                     in[12], in[13], (char*)d_ws, (char*)d_out);
}
```

```cpp
#include <hip/hip_runtime.h>
#include <stdint.h>
#if defined(__HIP_DEVICE_COMPILE__) && __HIP_DEVICE_COMPILE__
#pragma clang attribute push(__attribute__((target("no-packed-fp32-ops"))), apply_to = function)
#define NOPK_POP _Pragma("clang attribute pop")
#else
#define NOPK_POP
#endif

typedef unsigned short u16;
typedef unsigned int u32;
typedef unsigned long long u64;
typedef short s16x4 __attribute__((ext_vector_type(4)));
typedef __bf16 bf2 __attribute__((ext_vector_type(2)));
typedef __bf16 bf8 __attribute__((ext_vector_type(8)));
typedef float f2 __attribute__((ext_vector_type(2)));
typedef float f4 __attribute__((ext_vector_type(4)));
typedef float f16v __attribute__((ext_vector_type(16)));
typedef u32 v2u __attribute__((ext_vector_type(2)));
typedef u32 v4u __attribute__((ext_vector_type(4)));
typedef u32 v3u __attribute__((ext_vector_type(3)));
#define LAS __attribute__((address_space(3)))
#define GAS __attribute__((address_space(1)))
#define CAS __attribute__((address_space(4)))
#define DI __device__ __forceinline__
#define TID ((int)__builtin_amdgcn_workitem_id_x())
#define BID ((int)__builtin_amdgcn_workgroup_id_x())
#define PRIO(n) __builtin_amdgcn_s_setprio(n)
#define SEXIT 99
#define SX(n) if (SEXIT == n) { asm volatile("s_waitcnt vmcnt(0) lgkmcnt(0)" ::: "memory"); if (tid == 0) st_flag(ws + WS_SF + s * 128, tag); STAMP_FLUSH(256 + s); return; }
#define SCHED() __builtin_amdgcn_sched_barrier(0)

#define STAMP(i)
#define STAMP_DECL
#define STAMP_FLUSH(b)
#define COUNT(i)

constexpr u32 WS_EPOCH = 0;
constexpr u32 WS_G = 4096;
constexpr u32 WS_OF = 32768;
constexpr u32 WS_XF = 65536;
constexpr u32 WS_SF = 98304;
constexpr u32 WS_KVP = 1u << 20;
constexpr u32 WS_XT = 2u << 20;
constexpr u32 WS_O = 6u << 20;
constexpr u32 WS_BT = 8u << 20;
constexpr u32 WS_END = 10u << 20;
constexpr u32 SPIN_CAP = 1u << 16;

DI float u2f(u32 u) { return __builtin_bit_cast(float, u); }
DI u32 f2u(float f) { return __builtin_bit_cast(u32, f); }
DI bool anyl(bool p) { return __builtin_amdgcn_ballot_w64(p) != 0; }
DI float exp_acc(float x) {
  const float t = x * 1.4426950408889634f, r = __builtin_fmaf(x, 1.4426950408889634f, -t) + x * 1.925963033500011e-8f;
  const float e = __builtin_amdgcn_exp2f(t);
  return __builtin_fmaf(e, r * 0.6931471805599453f, e);
}
DI u32 pk2(float a, float b) { f2 v = {a, b}; return __builtin_bit_cast(u32, __builtin_convertvector(v, bf2)); }
DI u32 pk2(f2 v) { return __builtin_bit_cast(u32, __builtin_convertvector(v, bf2)); }
DI float blo(u32 u) { return u2f(u << 16); }
DI float bhi(u32 u) { return u2f(u & 0xffff0000u); }
DI f2 unpk(u32 u) { return f2{blo(u), bhi(u)}; }
DI float rbf(float a) { return blo(pk2(a, a)); }
DI float fmx(float a, float b) { return __builtin_elementwise_maximum(a, b); }
DI float dot2(u32 a, u32 b, float c) {
  return __builtin_amdgcn_fdot2_f32_bf16(__builtin_bit_cast(bf2, a), __builtin_bit_cast(bf2, b), c, false);
}
DI f16v mfma32(v4u a, v4u b, f16v c) {
  return __builtin_amdgcn_mfma_f32_32x32x16_bf16(__builtin_bit_cast(bf8, a), __builtin_bit_cast(bf8, b), c, 0, 0, 0);
}
DI f4 mfma16(v4u a, v4u b, f4 c) {
  return __builtin_amdgcn_mfma_f32_16x16x32_bf16(__builtin_bit_cast(bf8, a), __builtin_bit_cast(bf8, b), c, 0, 0, 0);
}
DI v2u trd(LAS char* p) { return __builtin_bit_cast(v2u, __builtin_amdgcn_ds_read_tr16_b64_v4i16((LAS s16x4*)p)); }
DI v4u trd2(LAS char* p0, LAS char* p1) { v2u a = trd(p0), b = trd(p1); return v4u{a.x, a.y, b.x, b.y}; }
DI v4u ld128(LAS char* p) { return *(LAS v4u*)p; }
DI v4u mksrd(const void* p, u32 bytes) { const u64 a = (u64)p; return v4u{(u32)a, (u32)(a >> 32) & 0xffffu, bytes, 0x00020000u}; }
#define DMA1(W, MOD, srd, voff, soff, lds) \
  asm volatile("s_mov_b32 m0, %0\n\ts_nop 0\n\tbuffer_load_" W " %1, %2, %3 offen " MOD " lds" ::"s"(lds), "v"(voff), "s"(srd), "s"(soff) : "m0", "memory")
#define DMA4(MOD, srd, v0, v1, v2, v3, soff, lds) \
  asm volatile("s_mov_b32 m0, %0\n\ts_nop 0\n\tbuffer_load_dwordx4 %1, %5, %6 offen " MOD " lds\n\t" \
               "buffer_load_dwordx4 %2, %5, %6 offen offset:1024 " MOD " lds\n\t" \
               "buffer_load_dwordx4 %3, %5, %6 offen offset:2048 " MOD " lds\n\t" \
               "buffer_load_dwordx4 %4, %5, %6 offen offset:3072 " MOD " lds" \
               ::"s"(lds), "v"(v0), "v"(v1), "v"(v2), "v"(v3), "s"(srd), "s"(soff) : "m0", "memory")
#define DMA2(MOD, srd, v0, v1, soff, lds) \
  asm volatile("s_mov_b32 m0, %0\n\ts_nop 0\n\tbuffer_load_dwordx4 %1, %3, %4 offen " MOD " lds\n\t" \
               "buffer_load_dwordx4 %2, %3, %4 offen offset:1024 " MOD " lds" \
               ::"s"(lds), "v"(v0), "v"(v1), "s"(srd), "s"(soff) : "m0", "memory")
#define WAITBAR(n) asm volatile("s_waitcnt vmcnt(" #n ") lgkmcnt(0)\n\ts_barrier" ::: "memory")
#define LBAR() asm volatile("s_waitcnt lgkmcnt(0)\n\ts_barrier" ::: "memory")
#define LWAIT() asm volatile("s_waitcnt lgkmcnt(0)" ::: "memory")
DI v4u pack8(const f16v& a, int o) {
  return v4u{pk2(a[o], a[o + 1]), pk2(a[o + 2], a[o + 3]), pk2(a[o + 4], a[o + 5]), pk2(a[o + 6], a[o + 7])};
}
DI __amdgpu_buffer_rsrc_t mkrsrc(const void* p, u32 bytes) {
  return __builtin_amdgcn_make_buffer_rsrc((void*)p, 0, bytes, 0x00020000);
}
DI void st16(v4u d, __amdgpu_buffer_rsrc_t r, u32 off) { __builtin_amdgcn_raw_buffer_store_b128(d, r, off, 0, 16); }
DI void st_flag(char* p, u32 v) { __hip_atomic_store((GAS u32*)p, v, __ATOMIC_RELAXED, __HIP_MEMORY_SCOPE_AGENT); }
DI void wait_flags(char* p, u32 tag) {
#pragma unroll 1
  for (u32 spins = 0; spins < SPIN_CAP; ++spins) {
    const u32 v = __hip_atomic_load((GAS u32*)p, __ATOMIC_RELAXED, __HIP_MEMORY_SCOPE_AGENT);
    if (!anyl(v != tag)) break;
    __builtin_amdgcn_s_sleep(2);
  }
  asm volatile("" ::: "memory");
}
DI void swap32(float& a, float& b) {
  auto r = __builtin_amdgcn_permlane32_swap(f2u(a), f2u(b), false, false);
  a = u2f(r[0]); b = u2f(r[1]);
}

#define PIN(x) asm volatile("" : "+v"(x))
struct Smx {
  float cp[2], cn[2]; f2 ep[2][4], en[2][4];
  static constexpr float L = 1.4426950408889634f;
  DI void step(int k, const f16v& a, v4u (&pos)[2], v4u (&neg)[2]) {
    if (k < 2) {
      const int o = 8 * k;
      cp[k] = fmx(fmx(fmx(fmx(a[o], a[o + 1]), a[o + 2]), fmx(fmx(a[o + 3], a[o + 4]), a[o + 5])), fmx(a[o + 6], a[o + 7]));
      cn[k] = fmx(fmx(fmx(fmx(-a[o], -a[o + 1]), -a[o + 2]), fmx(fmx(-a[o + 3], -a[o + 4]), -a[o + 5])), fmx(-a[o + 6], -a[o + 7]));
      PIN(cp[k]); PIN(cn[k]);
    } else if (k == 2) {
      swap32(cp[0], cn[0]); swap32(cp[1], cn[1]);
      cp[0] = fmx(cp[0], cn[0]) * -L; cp[1] = fmx(cp[1], cn[1]) * -L;
      cn[0] = cp[0]; cn[1] = cp[1];
      swap32(cp[0], cn[0]); swap32(cp[1], cn[1]);
      PIN(cp[0]); PIN(cn[0]); PIN(cp[1]); PIN(cn[1]);
    } else if (k < 11) {
      const int d = (k - 3) >> 2, i = (k - 3) & 3;
      const f2 p = {a[8 * d + 2 * i], a[8 * d + 2 * i + 1]};
      const f2 u = __builtin_elementwise_fma(p, f2{L, L}, f2{cp[d], cp[d]});
      const f2 v = __builtin_elementwise_fma(p, f2{-L, -L}, f2{cn[d], cn[d]});
      ep[d][i] = f2{__builtin_amdgcn_exp2f(u.x), __builtin_amdgcn_exp2f(u.y)};
      en[d][i] = f2{__builtin_amdgcn_exp2f(v.x), __builtin_amdgcn_exp2f(v.y)};
      PIN(ep[d][i]); PIN(en[d][i]);
    } else if (k < 13) {
      const int d = k - 11;
      const f2 s2 = (ep[d][0] + ep[d][1]) + (ep[d][2] + ep[d][3]), t2 = (en[d][0] + en[d][1]) + (en[d][2] + en[d][3]);
      cp[d] = s2.x + s2.y; cn[d] = t2.x + t2.y;
      PIN(cp[d]); PIN(cn[d]);
    } else if (k == 13) {
      swap32(cp[0], cn[0]); swap32(cp[1], cn[1]);
      cp[0] = __builtin_amdgcn_rcpf(cp[0] + cn[0]); cp[1] = __builtin_amdgcn_rcpf(cp[1] + cn[1]);
      PIN(cp[0]); PIN(cp[1]);
    } else if (k == 14) {
      cn[0] = cp[0]; cn[1] = cp[1];
      swap32(cp[0], cn[0]); swap32(cp[1], cn[1]);
      PIN(cp[0]); PIN(cn[0]); PIN(cp[1]); PIN(cn[1]);
    } else {
      const int d = (k - 15) >> 1;
      if ((k - 15) & 1) {
        const f2 r = {cn[d], cn[d]};
        neg[d] = v4u{pk2(en[d][0] * r), pk2(en[d][1] * r), pk2(en[d][2] * r), pk2(en[d][3] * r)};
        PIN(neg[d]);
      } else {
        const f2 r = {cp[d], cp[d]};
        pos[d] = v4u{pk2(ep[d][0] * r), pk2(ep[d][1] * r), pk2(ep[d][2] * r), pk2(ep[d][3] * r)};
        PIN(pos[d]);
      }
    }
  }
};
template <int COL, int AT>
DI void dmac(v4u srd, u32 voff, u32 soff, u32 base) {
  asm volatile("s_add_u32 m0, %0, %1\n\ts_nop 0\n\tbuffer_load_dwordx4 %2, %3, %4 offen offset:%5 lds"
               ::"s"(base), "i"(AT - COL), "v"(voff), "s"(srd), "s"(soff), "i"(COL) : "m0", "scc", "memory");
}
DI void dma4(v4u srd, u32 v0, u32 v1, u32 v2, u32 v3, u32 soff, u32 lds) { DMA4("", srd, v0, v1, v2, v3, soff, lds); }
DI u32 la(LAS char* p) { return (u32)(size_t)p; }
DI LAS char* lp(u32 a) { return (LAS char*)(size_t)a; }

template <int N> struct ic { static constexpr int value = N; };
constexpr int SLOT = 20480;
constexpr int G_X = 61440;
constexpr int G_B = 65536, G_G = 73728, G_KV = 77824;
constexpr int wv_at(int c) { return c < 5 ? G_X + 4096 * c : 2 * SLOT + 4096 * (c - 5); }
constexpr int wq_at(int c) { return c < 5 ? 4096 * c : c < 7 ? 53248 + 4096 * (c - 5) : 36864; }
#define GROUPS(n, a_, na, b_, nb) \
  _Pragma("unroll") for (int i_ = 0; i_ < n; ++i_) { __builtin_amdgcn_sched_group_barrier(a_, na, 0); __builtin_amdgcn_sched_group_barrier(b_, nb, 0); }
DI void attn_block(int b, const char* x, const char* wqkv, const char* wphi, const char* wo, char* ws, char* out,
                   LAS char* sm) {
  STAMP_DECL; STAMP(0);
  const int tid = TID, lane = tid & 63, w = __builtin_amdgcn_readfirstlane(tid >> 6);
  const int xcd = b & 7, j = b >> 3, a = j & 7, c = j >> 3;
  const int rt = (xcd >> 1) * 4 + c, hg = (xcd & 1) * 8 + a;
  const int rot = ((a & 1) + (c & 1) * 2) * 2;
  const int h = lane >> 5, r32 = lane & 31;
  const int G = lane >> 4, q4 = (lane & 15) >> 2, p4 = lane & 3, blk = G & 1;

  const v4u xd = mksrd(x, 2u << 20), wd = mksrd(wqkv, 1536u * 1024u);
  const u32 lb = (u32)(size_t)sm;
  const u32 xb = (rt * 128 + w * 32) * 1024;
  const u32 xo0 = (lane >> 3) * 1024 + (((lane & 7) ^ G) << 4);
  const u32 xo1 = (xo0 ^ 64) + 7168, xo2 = xo0 + 2 * 7168, xo3 = xo1 + 2 * 7168;
#define XDMA(s0) dma4(xd, xo0, xo1, xo2, xo3, xb + (((s0) + rot) & 7) * 128, lb + ((s0) % 3) * SLOT + w * 4096)
  XDMA(0);
  SCHED();
  const u32 wb = hg * 64 + 16 * w * 3072;
  const u32 wv = (lane >> 2) * 3072 + (lane & 3) * 16;
  const u32 lw = lb + w * 1024;
  u32 wc8[8];
  wc8[0] = wb + rot * (64 * 3072);
#define WDMA(s0, col, at) dmac<col, at>(wd, wv, wc8[s0], lw)
  WDMA(0, 1024, 16384);
  XDMA(1);
  SCHED();
#pragma unroll
  for (int i = 1; i < 8; ++i) wc8[i] = wb + ((i + rot) & 7) * (64 * 3072);
  WDMA(1, 1024, SLOT + 16384);
  {
    const v4u pd = mksrd(wphi, 16384u);
    DMA1("dword", "", pd, tid * 4, hg * 1024, lb + G_X + w * 256);
  }
  XDMA(2);
  PRIO(2);
  SCHED();
  const u32 tag = *(const CAS u32*)(ws + WS_EPOCH) + 1u;

  LAS char* xr[4];
#pragma unroll
  for (int ks = 0; ks < 4; ++ks) xr[ks] = sm + w * 4096 + r32 * 128 + (((2 * ks + h) ^ ((r32 >> 1) & 7)) << 4);
  LAS char* wr = sm + (8 * h + q4) * 64 + blk * 32 + p4 * 8;
  auto wfrag = [&](int at, int ks) { return trd2(wr + at + ks * 1024, wr + at + ks * 1024 + 256); };
  u32 a_row, a_va, a_pb0, a_pb1, a_vw;

  v4u xf[32];
  v4u wp0, wp1;
  f16v ak = {};
  u32 a_hi;
  auto kstep = [&](auto S0) {
    constexpr int s0 = S0.value;
    if (s0 == 0) WAITBAR(10);
    else if (s0 == 1) WAITBAR(9);
    else if (s0 <= 5) WAITBAR(9);
    else if (s0 == 6) WAITBAR(5);
    else WAITBAR(2);
    if (s0 == 0) STAMP(1);
    constexpr int so = (s0 % 3) * SLOT;
    if constexpr (s0 <= 5) WDMA(s0 + 2, 1024, ((s0 + 2) % 3) * SLOT + 16384);
    if constexpr (s0 == 6) { WDMA(0, 2048, wv_at(0)); WDMA(1, 2048, wv_at(1)); }
    v4u fk[4];
#pragma unroll
    for (int ks = 0; ks < 4; ++ks) { fk[ks] = wfrag(so + 16384, ks); xf[s0 * 4 + ks] = ld128(xr[ks] + so); }
    if constexpr (s0 == 7) { WDMA(2, 2048, wv_at(2)); WDMA(3, 2048, wv_at(3)); WDMA(4, 2048, wv_at(4)); WDMA(5, 2048, wv_at(5)); }
    if (s0 == 1) {
      LAS char* pp = sm + G_X + blk * 512 + (4 * h + q4) * 32 + p4 * 8;
      const v4u wph = trd2(pp, pp + 256), zero4 = {0, 0, 0, 0};
      wp0 = blk == 0 ? wph : zero4; wp1 = blk == 0 ? zero4 : wph;
      PIN(wp0); PIN(wp1);
    }
    if (s0 == 2) {
      const int hd = w & 1, fh = w >> 1;
      a_row = la(sm + SLOT + w * 4096 + r32 * 64 + ((h ^ ((r32 >> 1) & 7)) << 3));
      a_va = la(sm + G_X + (G >> 1) * 1024 + (blk * 32 + hd * 16 + (lane & 15)) * 16);
      a_vw = la(sm + G_X + w * 2048 + lane * 16);
      a_hi = la(wr + 40960);
      const int t0 = 16 * (G >> 1) + 4 * blk + q4;
      a_pb0 = la(sm + SLOT + hd * 2048 + t0 * 64 + (((4 * fh + p4) ^ ((t0 >> 1) & 7)) << 3));
      a_pb1 = la(sm + SLOT + hd * 2048 + (t0 + 8) * 64 + (((4 * fh + p4) ^ (((t0 + 8) >> 1) & 7)) << 3));
      PIN(a_row); PIN(a_va); PIN(a_pb0); PIN(a_pb1); PIN(a_vw); PIN(a_hi);
    }
    SCHED();
#pragma unroll
    for (int ks = 0; ks < 4; ++ks) ak = mfma32(fk[ks], xf[s0 * 4 + ks], ak);
    PIN(ak);
    SCHED();
    if constexpr (s0 <= 4) XDMA(s0 + 3);
  };
  kstep(ic<0>{}); kstep(ic<1>{}); kstep(ic<2>{}); kstep(ic<3>{}); kstep(ic<4>{}); kstep(ic<5>{}); kstep(ic<6>{}); kstep(ic<7>{});
  STAMP(2);
  {
    f16v pk = {}, av = {};
    pk = mfma32(wp0, pack8(ak, 0), pk);
    pk = mfma32(wp1, pack8(ak, 8), pk);
    PIN(pk);
    v4u fv[4];
    auto pre = [&](int n) {
      LAS char* p = lp(a_hi) + (wv_at(n >> 2) - 40960 + (n & 3) * 1024);
      fv[n & 3] = trd2(p, p + 256);
    };
    auto vm = [&](int n) { av = mfma32(xf[n], fv[n & 3], av); PIN(av); };
    Smx m;
    v4u pos[2], neg[2];
    WAITBAR(4);
    STAMP(12);
    pre(0); pre(1); pre(2); pre(3);
    WDMA(6, 2048, wv_at(6)); WDMA(7, 2048, wv_at(7));
    SCHED();
    m.step(0, pk, pos, neg); m.step(1, pk, pos, neg);
    SCHED();
#pragma unroll
    for (int n = 0; n < 8; ++n) {
      vm(n);
      if (n < 4) pre(n + 4);
      SCHED();
      m.step(2 + n, pk, pos, neg);
      SCHED();
    }
    WAITBAR(2);
    STAMP(13);
    pre(8); pre(9); pre(10); pre(11);
    WDMA(0, 0, wq_at(0)); WDMA(1, 0, wq_at(1)); WDMA(2, 0, wq_at(2));
    SCHED();
    m.step(10, pk, pos, neg);
    SCHED();
#pragma unroll
    for (int n = 8; n < 24; ++n) {
      vm(n);
      if (n < 20) pre(n + 4);
      SCHED();
      if (!(n & 1)) m.step(11 + ((n - 8) >> 1), pk, pos, neg);
      if (n == 19) { *(LAS v2u*)lp(a_row) = v2u{pos[0].x, pos[0].y}; *(LAS v2u*)lp(a_row ^ 16) = v2u{pos[0].z, pos[0].w}; }
      if (n == 21) { *(LAS v2u*)lp(a_row ^ 32) = v2u{neg[0].x, neg[0].y}; *(LAS v2u*)lp(a_row ^ 48) = v2u{neg[0].z, neg[0].w}; }
      if (n == 23) { *(LAS v2u*)(lp(a_row) + 2048) = v2u{pos[1].x, pos[1].y}; *(LAS v2u*)(lp(a_row ^ 16) + 2048) = v2u{pos[1].z, pos[1].w}; }
      SCHED();
    }
    WAITBAR(3);
    STAMP(14);
    pre(24); pre(25); pre(26); pre(27);
    *(LAS v2u*)(lp(a_row ^ 32) + 2048) = v2u{neg[1].x, neg[1].y}; *(LAS v2u*)(lp(a_row ^ 48) + 2048) = v2u{neg[1].z, neg[1].w};
    WDMA(3, 0, wq_at(3)); WDMA(4, 0, wq_at(4));
    SCHED();
#pragma unroll
    for (int n = 24; n < 32; ++n) {
      vm(n);
      if (n < 28) pre(n + 4);
      if (n == 28) WDMA(5, 0, wq_at(5));
      if (n == 29) WDMA(6, 0, wq_at(6));
      if (n == 30) WDMA(7, 0, wq_at(7));
      SCHED();
    }
    *(LAS v4u*)lp(a_vw) = pack8(av, 0);
    *(LAS v4u*)(lp(a_vw) + 1024) = pack8(av, 8);
  }
  LBAR();
  STAMP(15);
  const __amdgpu_buffer_rsrc_t kvr = mkrsrc(ws + WS_KVP + hg * 65536, 65536);
  {
    v4u fa[4], fb[4];
#pragma unroll
    for (int t = 0; t < 4; ++t) { fb[t] = trd2(lp(a_pb0) + t * 4096, lp(a_pb1) + t * 4096); fa[t] = ld128(lp(a_va) + t * 2048); }
    SCHED();
    const v4u ones = {0x3f803f80u, 0x3f803f80u, 0x3f803f80u, 0x3f803f80u};
    f4 kv = {}, z = {};
#pragma unroll
    for (int t = 0; t < 4; ++t) { z = mfma16(ones, fb[t], z); kv = mfma16(fa[t], fb[t], kv); }
    const u32 zt = __builtin_amdgcn_perm(pk2(z[0], z[0]), tag, 0x07060100u);
    st16(v4u{pk2(kv[0], kv[1]), tag, pk2(kv[2], kv[3]), zt}, kvr, tid * 16 + rt * 4096);
  }
  STAMP(3);
  WAITBAR(1);
  STAMP(16);
  f16v aq = {};
  v4u rec[16];
  const u32 ro = tid * 16;
  {
    v4u fq[8];
    auto pre = [&](int n) {
      const int at = wq_at(n >> 2) + (n & 3) * 1024;
      fq[n & 7] = trd2(wr + at, wr + at + 256);
    };
#pragma unroll
    for (int n = 0; n < 6; ++n) pre(n);
#pragma unroll
    for (int n = 0; n < 32; n += 2) {
      SCHED();
      aq = mfma32(fq[n & 7], xf[n], aq); PIN(aq);
      aq = mfma32(fq[(n + 1) & 7], xf[n + 1], aq); PIN(aq);
      if (n < 26) { pre(n + 6); pre(n + 7); }
      if (n >= 26) rec[(n - 26) >> 1] = __builtin_amdgcn_raw_buffer_load_b128(kvr, ro, ((n - 26) >> 1) * 4096, 16);
    }
    SCHED();
  }
  STAMP(4);
  v4u qp[2], qn[2];
  {
    rec[3] = __builtin_amdgcn_raw_buffer_load_b128(kvr, ro, 3 * 4096, 16);
    SCHED();
    f16v pq = {};
    pq = mfma32(wp0, pack8(aq, 0), pq);
    pq = mfma32(wp1, pack8(aq, 8), pq);
    PIN(pq);
    LBAR();
    Smx m;
#pragma unroll
    for (int k = 0; k < 19; ++k) {
      if (k < 12) rec[4 + k] = __builtin_amdgcn_raw_buffer_load_b128(kvr, ro, (4 + k) * 4096, 16);
      SCHED();
      m.step(k, pq, qp, qn);
      SCHED();
    }
  }
  STAMP(5);
  const int tb = (xcd >> 1) * 8 + (xcd & 1) * 4 + c, cb = a, ro4 = a & 3;
  float s0, s1, s2, s3, sz;
  {
    const v4u od = mksrd(wo, 512u * 1024u);
    const u32 so0 = (lane >> 3) * 1024 + (((lane & 7) ^ (blk << 2)) << 4);
    const u32 so1 = so0 + 7168, so2 = so0 + 2 * 7168, so3 = so0 + 3 * 7168;
    auto stale = [&]() {
      u32 b1 = rec[0].y ^ tag, b2 = rec[0].w ^ tag;
#pragma unroll
      for (int p = 1; p < 16; ++p) {
        b1 = __builtin_amdgcn_bitop3_b32(b1, rec[p].y, tag, 0xf6);
        b2 = __builtin_amdgcn_bitop3_b32(b2, rec[p].w, tag, 0xf6);
      }
      return anyl(__builtin_amdgcn_bitop3_b32(b1, b2, 0xffffu, 0xf8) != 0);
    };
    auto add4 = [&](int g) {
#pragma unroll
      for (int p = g; p < g + 4; ++p) {
        s0 += blo(rec[p].x); s1 += bhi(rec[p].x); s2 += blo(rec[p].z); s3 += bhi(rec[p].z);
        sz += bhi(rec[p].w);
      }
    };
    if (__builtin_expect(stale(), 0)) {
#pragma unroll 1
      for (u32 spins = 0; spins < SPIN_CAP; ++spins) {
        COUNT(19);
        asm volatile("" ::: "memory");
#pragma unroll
        for (int p = 0; p < 16; ++p) rec[p] = __builtin_amdgcn_raw_buffer_load_b128(kvr, ro, p * 4096, 16);
        if (!stale()) break;
      }
    }
    s0 = s1 = s2 = s3 = sz = 0.f;
#pragma unroll
    for (int i = 0; i < 4; ++i) {
      SCHED();
      dma4(od, so0, so1, so2, so3, cb * 128 + (((i + ro4) & 3) * 4 + w) * 32768, lb + (i * 4 + w) * 4096);
      SCHED();
      add4(4 * i);
      PIN(s0); PIN(s1); PIN(s2); PIN(s3); PIN(sz);
    }
  }
  STAMP(6);
  {
    LAS char* p = sm + G_KV + (w & 1) * 2048 + ((w >> 1) * 16 + (lane & 15)) * 64 + G * 8;
    const u32 zz = pk2(sz, sz);
    *(LAS v2u*)p = v2u{pk2(s0, s1), pk2(s2, s3)};
    *(LAS v2u*)(p + 32) = v2u{zz, zz};
  }
  LBAR();
  const __amdgpu_buffer_rsrc_t wsr = mkrsrc(ws, WS_END);
  {
    LAS char* im = sm + G_KV + (4 * h + q4) * 64 + blk * 32 + p4 * 8;
    v4u f[4];
#pragma unroll
    for (int i = 0; i < 4; ++i) f[i] = trd2(im + i * 1024, im + i * 1024 + 512);
    f16v n0 = {}, n1 = {};
    n0 = mfma32(f[0], qp[0], n0); n1 = mfma32(f[2], qp[1], n1);
    n0 = mfma32(f[1], qn[0], n0); n1 = mfma32(f[3], qn[1], n1);
    const float i0 = __builtin_amdgcn_rcpf(n0[8] + 1e-6f), i1 = __builtin_amdgcn_rcpf(n1[8] + 1e-6f);
    const f2 j0 = {i0, i0}, j1 = {i1, i1};
    const u32 oo = WS_O + ((rt * 4 + w) * 32 + hg * 2) * 1024;
    st16(v4u{pk2(f2{n0[0], n0[1]} * j0), pk2(f2{n0[2], n0[3]} * j0), pk2(f2{n0[4], n0[5]} * j0), pk2(f2{n0[6], n0[7]} * j0)}, wsr, lane * 16 + oo);
    st16(v4u{pk2(f2{n1[0], n1[1]} * j1), pk2(f2{n1[2], n1[3]} * j1), pk2(f2{n1[4], n1[5]} * j1), pk2(f2{n1[6], n1[7]} * j1)}, wsr, lane * 16 + oo + 1024);
  }
  STAMP(7);
  WAITBAR(0);
  if (tid == 0) st_flag(ws + WS_OF + (rt * 16 + hg) * 128, tag);
  STAMP(8);
  const int wc = w & 1, wt = w >> 1;
  v4u wf[32];
  {
    LAS char* wr2 = sm + (4 * h + q4) * 128 + (((4 * wc + 2 * blk + (p4 >> 1)) ^ ((q4 >> 1) << 2)) << 4) + (p4 & 1) * 8;
#pragma unroll
    for (int i = 0; i < 32; ++i) wf[i] = trd2(wr2 + i * 2048, wr2 + i * 2048 + 1024);
  }
  STAMP(17);
  if (w == 0) {
    const u32 fo = lane < 16 ? WS_OF + ((tb >> 1) * 16 + lane) * 128
                 : WS_SF + 128 * (lane < 48 ? cb * 32 + lane - 16 : (lane < 56 || b != 0) ? tb * 8 + (lane & 7) : (lane & 7) * 32);
    wait_flags(ws + fo, tag);
  }
  LBAR();
  STAMP(9);
  const v4u sd = mksrd(ws, WS_END);
  {
    const u32 lo = lane * 16;
    const u32 ob = WS_O + ((tb * 2 + wt) * 32 + wc * 4) * 1024;
#pragma unroll
    for (int s = 0; s < 4; ++s) DMA4("sc1", sd, lo, lo, lo, lo, ob + ((s + ro4) & 3) * 8192, lb + s * 16384 + w * 4096);
    const u32 bo = (lane >> 3) * 4096 + (((lane & 7) ^ (blk << 2)) << 4);
    DMA2("sc1", sd, bo, bo + 31744, WS_BT + (cb * 64 + w * 16) * 4096 + tb * 128, lb + G_B + w * 2048);
    DMA1("dword", "sc1", sd, lane * 4, WS_G + tb * 256, lb + G_G + w * 256);
  }
  if (b == 0 && tid == 0) st_flag(ws + WS_EPOCH, tag);
  f16v acc = {};
  {
    LAS char* orr = sm + wt * 8192 + lane * 16;
#pragma unroll
    for (int s = 0; s < 4; ++s) {
      if (s == 0) WAITBAR(15);
      else if (s == 1) WAITBAR(11);
      else if (s == 2) WAITBAR(7);
      else WAITBAR(0);
      if (s == 0) STAMP(18);
      v4u fo[8];
#pragma unroll
      for (int i = 0; i < 8; ++i) fo[i] = ld128(orr + s * 16384 + i * 1024);
      SCHED();
#pragma unroll
      for (int i = 0; i < 8; ++i) acc = mfma32(wf[s * 8 + i], fo[i], acc);
    }
  }
  STAMP(10);
  {
    const int t = wt * 32 + r32;
    const float g = *(LAS float*)(sm + G_G + t * 4), g1 = rbf(1.f - g);
    LAS char* br = sm + G_B + (32 * wc + 4 * h + q4) * 128 + (((4 * wt + 2 * blk + (p4 >> 1)) ^ ((q4 >> 1) << 2)) << 4) + (p4 & 1) * 8;
    const f2 gg = {g, g}, gh = {g1, g1};
    v2u o[4];
#pragma unroll
    for (int k = 0; k < 4; ++k) {
      const v2u bv = trd(br + k * 1024);
      o[k] = v2u{pk2(__builtin_elementwise_fma(f2{acc[4 * k], acc[4 * k + 1]}, gg, gh * unpk(bv.x))),
                 pk2(__builtin_elementwise_fma(f2{acc[4 * k + 2], acc[4 * k + 3]}, gg, gh * unpk(bv.y)))};
    }
    char* op = out + (size_t)(tb * 64 + t) * 1024 + cb * 128 + wc * 64 + h * 16;
#pragma unroll
    for (int k = 0; k < 4; k += 2) {
      auto rx = __builtin_amdgcn_permlane32_swap(o[k].x, o[k + 1].x, false, false);
      auto ry = __builtin_amdgcn_permlane32_swap(o[k].y, o[k + 1].y, false, false);
      *(GAS v4u*)(op + k * 16) = v4u{rx[0], ry[0], rx[1], ry[1]};
    }
  }
  STAMP(11);
  STAMP_FLUSH(b);
}

DI f2 cmul(f2 a, f2 b) { return f2{__builtin_fmaf(a.x, b.x, -(a.y * b.y)), __builtin_fmaf(a.x, b.y, a.y * b.x)}; }
DI u32 pkc(f2 a) { return pk2(a.x, a.y); }
DI u32 pkn(f2 a) { return pk2(a.x, -a.y); }
template <int OFF>
DI void ew(u32 v) { asm volatile("ds_write_addtid_b32 %0 offset:%1" ::"v"(v), "i"(OFF) : "memory"); }
DI f2 cexp2(float lr, float li, float t) {
  const float e = __builtin_amdgcn_exp2f(lr * t), ph = __builtin_amdgcn_fractf(li * t);
  return f2{e, e} * f2{__builtin_amdgcn_cosf(ph), __builtin_amdgcn_sinf(ph)};
}
constexpr u32 kc_index(int lag) {
  if (lag < 0) return 768;
  const int sh = lag < 256 ? 0 : lag < 512 ? 1 : lag < 1024 ? 2 : 3;
  const int q = 1 << sh, m = lag >> sh, rem = lag & (q - 1);
  const int v = (m + ((2 * rem > q || (2 * rem == q && (m & 1))) ? 1 : 0)) << sh;
  const int col = v & 0xf0;
  return v < 256    ? v
         : v < 512  ? 256 + col + ((v & 2) ? 12 : 0) + ((v >> 2) & 3)
         : v < 1024 ? 256 + col + 4 + ((v >> 6) & 4) + ((v >> 2) & 3)
         : v < 2048 ? 512 + col + (v & 8) + ((v >> 8) & 3)
                    : 516;
}
struct KcTab { u32 v[1024]; };
constexpr KcTab make_kc() {
  KcTab t = {};
  for (int i = 0; i < 256; ++i)
    for (int e = 0; e < 9; ++e) t.v[4 * i + e / 3] |= kc_index(2047 - 8 * i - e) << (1 + 10 * (e % 3));
  return t;
}
__device__ __attribute__((aligned(16))) constexpr KcTab kc_tab = make_kc();

constexpr int E_IMG = 8448;
constexpr int S_TBL = 0, TBL_CH = 8448, TBL_ODD = 4160;
constexpr int S_RED = 20480;
constexpr int S_U = 69632, U_CH = 4576;
constexpr int S_KC = 78784, KC_CH = 1552;

DI void ssm_block(int s, const char* x, const char* Are, const char* Aim, const char* Bre, const char* Bim,
                  const char* Cre, const char* Cim, const char* logdt, const char* dsk, const char* gw, char* ws,
                  LAS char* sm) {
  STAMP_DECL; STAMP(0);
  const int tid = TID, lane = tid & 63, w = __builtin_amdgcn_readfirstlane(tid >> 6);
  const int grp = s >> 5, ta = s & 31;
  const int G = lane >> 4, q4 = (lane & 15) >> 2, p4 = lane & 3, ci = lane & 15;
  const int ch = w >> 1;
  const u32 lb = (u32)(size_t)sm;
  PRIO(3);
  {
    const v4u xd = mksrd(x, 2u << 20);
    const u32 xo = (lane >> 3) * 1024 + (((lane & 7) ^ (2 * G)) << 4);
    DMA2("", xd, xo, xo + 7168, (ta * 64 + w * 16) * 1024 + grp * 128, lb + w * 2048);
  }
  {
    const char* pa[6] = {Are, Aim, Bre, Bim, Cre, Cim};
#pragma unroll
    for (int i = 0; i < 3; ++i) {
      const v4u d = mksrd(w < 2 ? pa[2 * i] : pa[2 * i + 1], 131072u);
      DMA1("dword", "", d, lane * 4, s * 512 + (w & 1) * 256, lb + 16384 + i * 1024 + w * 256);
    }
  }
  const u32 ldt2 = *(const CAS u32*)(logdt + s * 4), dsk2 = *(const CAS u32*)(dsk + s * 4);
  SCHED();
  const u32 tag = *(const CAS u32*)(ws + WS_EPOCH) + 1u;
  const __amdgpu_buffer_rsrc_t wsr = mkrsrc(ws, WS_END);
  {
    v2u v[4];
    if (tid < 2) *(LAS u32*)(sm + S_KC + tid * KC_CH + 1536) = 0;
    if (tid >= 64 && tid < 124) {
      const int i = tid - 64;
      *(LAS v4u*)(sm + S_U + (i >= 30 ? U_CH + (i - 30) * 16 : i * 16)) = v4u{0, 0, 0, 0};
    }
    WAITBAR(0);
    STAMP(1);
    SX(1);
    LAS char* im2 = sm + 8192 + w * 2048;
    LAS char* rp = sm + (16 * G + q4) * 128 + (p4 & 1) * 8;
#pragma unroll
    for (int k = 0; k < 4; ++k) v[k] = trd(rp + k * 512 + (((2 * w + (p4 >> 1)) ^ (2 * ((2 * k + (q4 >> 1)) & 3))) << 4));
    *(LAS v4u*)(im2 + ci * 128 + (((2 * G) ^ (ci & 7)) << 4)) = v4u{v[0].x, v[0].y, v[1].x, v[1].y};
    *(LAS v4u*)(im2 + ci * 128 + (((2 * G + 1) ^ (ci & 7)) << 4)) = v4u{v[2].x, v[2].y, v[3].x, v[3].y};
    LWAIT();
    const int c = lane >> 3;
    LAS char* r2 = im2 + c * 128 + (((lane & 7) ^ c) << 4);
    const v4u a0 = ld128(r2), a1 = ld128(r2 + 1024);
    const u32 vo = WS_XT + (grp * 64 + w * 16 + c) * 4096 + ta * 128 + (lane & 7) * 16;
    st16(a0, wsr, vo);
    st16(a1, wsr, vo + 8 * 4096);
  }
  PRIO(0);
  LAS u16* pp = (LAS u16*)(sm + 16384) + tid;
  const u32 are = pp[0], aim = pp[256], bre = pp[512], bim = pp[768], cre = pp[1024], cim = pp[1280];
  LBAR();
  STAMP(2);
  SX(2);
  v3u kt; v4u g0, g1, gwv;
  {
    const float dt = rbf(exp_acc(ch ? bhi(ldt2) : blo(ldt2)));
    const float lr = (dt * blo(are)) * 1.4426950408889634f, li = (dt * blo(aim)) * 0.15915494309189535f;
    const f2 W = cmul(f2{blo(cre), blo(cim)}, f2{dt * blo(bre), dt * blo(bim)});
    asm volatile("s_mov_b32 m0, %0" ::"s"(lb + ch * 4 * E_IMG + (w & 1) * 256) : "m0");
    u32 ev[4][16];
#define EW(t, r, val) ev[t][r] = (val)
    f2 e[16];
    e[1] = cexp2(lr, li, 1.f);
    const f2 f1 = cexp2(lr, li, 16.f), c1 = cexp2(lr, li, 256.f);
    e[2] = cmul(e[1], e[1]);
    const f2 f2_ = cmul(f1, f1), c2 = cmul(c1, c1);
    e[3] = cmul(e[2], e[1]); e[4] = cmul(e[2], e[2]);
    const f2 f4_ = cmul(f2_, f2_), c3 = cmul(c2, c1), c4 = cmul(c2, c2);
    f2 a[16];
    a[0] = W; a[1] = cmul(W, f1); a[2] = cmul(W, f2_);
    e[5] = cmul(e[4], e[1]); e[6] = cmul(e[4], e[2]); e[7] = cmul(e[4], e[3]); e[8] = cmul(e[4], e[4]);
    a[3] = cmul(a[1], f2_);
    const f2 f8_ = cmul(f4_, f4_);
    EW(1, 0, 0x00003f80u);
    EW(1, 1, pkc(e[1])); EW(1, 2, pkc(e[2])); EW(1, 3, pkc(e[3])); EW(1, 4, pkc(e[4]));
    EW(0, 0, pkn(a[0])); EW(0, 1, pkn(a[1])); EW(0, 2, pkn(a[2])); EW(0, 3, pkn(a[3]));
    EW(1, 5, pkc(e[5])); EW(1, 6, pkc(e[6])); EW(1, 7, pkc(e[7])); EW(1, 8, pkc(e[8]));
#pragma unroll
    for (int i = 0; i < 4; ++i) a[4 + i] = cmul(a[i], f4_);
    EW(0, 4, pkn(a[4])); EW(0, 5, pkn(a[5])); EW(0, 6, pkn(a[6])); EW(0, 7, pkn(a[7]));
    e[12] = cmul(e[8], e[4]);
    EW(1, 9, pkc(cmul(e[8], e[1]))); EW(1, 10, pkc(cmul(e[8], e[2]))); EW(1, 11, pkc(cmul(e[8], e[3]))); EW(1, 12, pkc(e[12]));
    EW(1, 13, pkc(cmul(e[8], e[5]))); EW(1, 14, pkc(cmul(e[8], e[6]))); EW(1, 15, pkc(cmul(e[8], e[7])));
    EW(0, 8, pkn(cmul(a[0], f8_))); EW(0, 9, pkn(cmul(a[1], f8_))); EW(0, 10, pkn(cmul(a[2], f8_))); EW(0, 11, pkn(cmul(a[3], f8_)));
    EW(0, 12, pkn(cmul(a[4], f8_))); EW(0, 13, pkn(cmul(a[5], f8_))); EW(0, 14, pkn(cmul(a[6], f8_))); EW(0, 15, pkn(cmul(a[7], f8_)));
    const f2 cp = cmul(c1, e[2]);
#define EW4(g, gv) EW(2, 4 * g, pkc(gv)); EW(2, 4 * g + 1, pkc(cmul(gv, e[4]))); EW(2, 4 * g + 2, pkc(cmul(gv, e[8]))); EW(2, 4 * g + 3, pkc(cmul(gv, e[12])))
    EW4(0, c1); EW4(1, c2); EW4(2, c3); EW4(3, cp);
    const f2 d1 = cmul(c4, c1), d2 = cmul(c4, c2), d3 = cmul(c4, c3);
    EW(3, 0, pkc(c4)); EW(3, 1, pkc(d1)); EW(3, 2, pkc(d2)); EW(3, 3, pkc(d3));
    EW(3, 8, pkc(cmul(c4, e[8]))); EW(3, 9, pkc(cmul(d1, e[8]))); EW(3, 10, pkc(cmul(d2, e[8]))); EW(3, 11, pkc(cmul(d3, e[8])));
    EW(3, 4, pkc(cmul(c4, c4)));
#define EP(t, r) PIN(ev[t][r])
#define EO(t, r) ew<(t) * E_IMG + (r) * 528>(ev[t][r])
#define E4(M, t, r) M(t, r); M(t, r + 1); M(t, r + 2); M(t, r + 3)
#define EALL(M) E4(M, 0, 0); E4(M, 0, 4); E4(M, 0, 8); E4(M, 0, 12); E4(M, 1, 0); E4(M, 1, 4); E4(M, 1, 8); E4(M, 1, 12); \
                E4(M, 2, 0); E4(M, 2, 4); E4(M, 2, 8); E4(M, 2, 12); E4(M, 3, 0); M(3, 4); E4(M, 3, 8)
    EALL(EP);
    WAITBAR(0);
    if (tid == 0) st_flag(ws + WS_XF + s * 128, tag);
    STAMP(9);
    kt = *(const GAS v3u*)((const char*)kc_tab.v + tid * 16);
    const char* gx = x + (size_t)(s * 8 + w * 2) * 1024 + lane * 16;
    g0 = *(const GAS v4u*)gx; g1 = *(const GAS v4u*)(gx + 1024); gwv = *(const GAS v4u*)(gw + lane * 16);
    EALL(EO);
  }
  if (w == 0) wait_flags(ws + WS_XF + (grp * 32 + (lane & 31)) * 128, tag);
  LBAR();
  {
    const v4u sd = mksrd(ws, WS_END);
    DMA2("sc1", sd, lane * 16, lane * 16, WS_XT + (2 * s + ch) * 4096 + (w & 1) * 2048, lb + S_U + ch * U_CH + 480 + (w & 1) * 2048);
  }
  STAMP(3);
  SX(3);
  {
    const int c2 = w & 1, tl = w >> 1;
    LAS char* eb = sm + c2 * 4 * E_IMG + ci * 528 + G * 16;
    LAS char* et = eb + (1 + tl) * E_IMG;
    LAS char* kc = sm + S_KC + c2 * KC_CH + ci * 32 + G * 8;
    v4u fa[8], fb[8];
#pragma unroll
    for (int ks = 0; ks < 8; ++ks) { fa[ks] = ld128(eb + ks * 64); fb[ks] = ld128(et + ks * 64); }
    SCHED();
    f4 k0 = {}, k1 = {};
#pragma unroll
    for (int ks = 0; ks < 4; ++ks) { k0 = mfma16(fb[ks], fa[ks], k0); k1 = mfma16(fb[4 + ks], fa[4 + ks], k1); }
    if (tl == 0) {
#pragma unroll
      for (int ks = 0; ks < 8; ++ks) fb[ks] = ld128(eb + 3 * E_IMG + ks * 64);
      SCHED();
      f4 k2 = {}, k3 = {};
#pragma unroll
      for (int ks = 0; ks < 4; ++ks) { k2 = mfma16(fb[ks], fa[ks], k2); k3 = mfma16(fb[4 + ks], fa[4 + ks], k3); }
      k2 += k3;
      *(LAS v2u*)(kc + 1024) = v2u{pk2(k2[0], k2[1]), pk2(k2[2], k2[3])};
    }
    k0 += k1;
    *(LAS v2u*)(kc + tl * 512) = v2u{pk2(k0[0], k0[1]), pk2(k0[2], k0[3])};
  }
  LBAR();
  STAMP(4);
  {
    const u32 ki[9] = {kt.x & 0x7feu, (kt.x >> 10) & 0x7feu, (kt.x >> 20) & 0x7feu, kt.y & 0x7feu, (kt.y >> 10) & 0x7feu, (kt.y >> 20) & 0x7feu,
                       kt.z & 0x7feu, (kt.z >> 10) & 0x7feu, (kt.z >> 20) & 0x7feu};
    u32 kv[2][9];
#pragma unroll
    for (int c2 = 0; c2 < 2; ++c2)
#pragma unroll
      for (int e = 0; e < 9; ++e) kv[c2][e] = *(LAS u16*)(sm + S_KC + c2 * KC_CH + ki[e]);
    SCHED();
#pragma unroll
    for (int c2 = 0; c2 < 2; ++c2) {
      LAS char* tb = sm + S_TBL + c2 * TBL_CH + tid * 16;
      const u32* k = kv[c2];
      *(LAS v4u*)tb = v4u{k[0] | (k[1] << 16), k[2] | (k[3] << 16), k[4] | (k[5] << 16), k[6] | (k[7] << 16)};
      *(LAS v4u*)(tb + TBL_ODD) = v4u{k[1] | (k[2] << 16), k[3] | (k[4] << 16), k[5] | (k[6] << 16), k[7] | (k[8] << 16)};
    }
    if (tid < 8) *(LAS v4u*)(sm + S_TBL + (tid >> 2) * TBL_CH + ((tid >> 1) & 1) * TBL_ODD + 4096 + (tid & 1) * 16) = v4u{0, 0, 0, 0};
  }
  {
    float a0 = 0, a1 = 0;
#pragma unroll
    for (int i = 0; i < 4; ++i) { a0 = dot2(g0[i], gwv[i], a0); a1 = dot2(g1[i], gwv[i], a1); }
    swap32(a0, a1);
    float v = a0 + a1;
    v += u2f(__builtin_amdgcn_mov_dpp(f2u(v), 0xB1, 0xf, 0xf, true));
    v += u2f(__builtin_amdgcn_mov_dpp(f2u(v), 0x4E, 0xf, 0xf, true));
    v += u2f(__builtin_amdgcn_mov_dpp(f2u(v), 0x141, 0xf, 0xf, true));
    v += u2f(__builtin_amdgcn_mov_dpp(f2u(v), 0x140, 0xf, 0xf, true));
    {
      auto r = __builtin_amdgcn_permlane16_swap(f2u(v), f2u(v), false, false);
      v = u2f(r[0]) + u2f(r[1]);
    }
    v = rbf(__builtin_amdgcn_rcpf(1.f + __builtin_amdgcn_exp2f(-1.4426950408889634f * rbf(v))));
    if ((lane & 31) == 0) __builtin_amdgcn_raw_buffer_store_b32(f2u(v), wsr, (lane >> 5) * 4, WS_G + (s * 8 + w * 2) * 4, 16);
  }
  WAITBAR(1);
  STAMP(5);
  SX(5);
  const int rh = w & 1;
  f4 acc[8] = {};
  {
    LAS char* ub = sm + S_U + ch * U_CH + ci * 32 + G * 16 + (4 - 4 * rh) * 64;
    const int s0 = 2031 + 8 * G - ci;
    u32 kb[4];
#pragma unroll
    for (int i = 0; i < 4; ++i) {
      kb[i] = lb + S_TBL + ch * TBL_CH + ((s0 & 1) ? TBL_ODD : 0) + (s0 >> 1) * 4 - rh * 256 - 59 * 64 + i * 1024;
      asm volatile("" : "+v"(kb[i]));
    }
    v4u bf[2][8], af[2][8];
    auto ld = [&](int rr) {
#pragma unroll
      for (int J = 0; J < 8; ++J) {
        bf[rr & 1][J] = ld128(ub + J * 512 + (3 - rr) * 64);
        LAS u32* kp = (LAS u32*)(size_t)kb[(7 - J) >> 1] + ((7 - J) & 1) * 128 + (3 - rr) * 16;
        af[rr & 1][J] = v4u{kp[0], kp[1], kp[2], kp[3]};
      }
    };
    ld(0);
#pragma unroll
    for (int rr = 0; rr < 4; ++rr) {
      SCHED();
      if (rr < 3) ld(rr + 1);
#pragma unroll
      for (int J = 0; J < 8; ++J)
#pragma unroll
        for (int i = J; i < 8; ++i) acc[i] = mfma16(af[rr & 1][J], bf[rr & 1][i - J], acc[i]);
      if (rr < 3) {
#pragma unroll
        for (int i = 0; i < 24; ++i) {
          __builtin_amdgcn_sched_group_barrier(0x8, 1, 0);
          __builtin_amdgcn_sched_group_barrier(0x100, 1, 0);
        }
        __builtin_amdgcn_sched_group_barrier(0x8, 12, 0);
      }
      SCHED();
    }
  }
  STAMP(6);
  {
    LAS char* mine = sm + S_RED + w * 8192 + lane * 16;
#pragma unroll
    for (int i = 0; i < 8; ++i) *(LAS f4*)(mine + i * 1024) = acc[i];
    LBAR();
    const float dk = ch ? bhi(dsk2) : blo(dsk2);
    const f2 dd = {dk, dk};
    LAS char* pa = sm + S_RED + ch * 16384 + (4 * rh + (lane >> 5)) * 1024 + (lane & 1) * 512 + ((lane >> 1) & 15) * 16;
    LAS char* up = sm + S_U + ch * U_CH + 480 + rh * 2048 + lane * 16;
    const u32 so = WS_BT + (2 * s + ch) * 4096 + rh * 2048;
#pragma unroll
    for (int i = 0; i < 2; ++i) {
      const f4 y0 = *(LAS f4*)(pa + i * 2048) + *(LAS f4*)(pa + i * 2048 + 8192);
      const f4 y1 = *(LAS f4*)(pa + i * 2048 + 256) + *(LAS f4*)(pa + i * 2048 + 8192 + 256);
      const v4u u = ld128(up + i * 1024);
      const v4u r = {pk2(__builtin_elementwise_fma(unpk(u.x), dd, f2{y0[0], y0[1]})), pk2(__builtin_elementwise_fma(unpk(u.y), dd, f2{y0[2], y0[3]})),
                     pk2(__builtin_elementwise_fma(unpk(u.z), dd, f2{y1[0], y1[1]})), pk2(__builtin_elementwise_fma(unpk(u.w), dd, f2{y1[2], y1[3]}))};
      st16(r, wsr, lane * 16 + so + i * 1024);
    }
  }
  STAMP(7);
  WAITBAR(0);
  if (tid == 0) st_flag(ws + WS_SF + s * 128, tag);
  STAMP(8);
  STAMP_FLUSH(256 + s);
}

__global__ __launch_bounds__(256, 2) void k_all(const char* x, const char* wqkv, const char* wphi, const char* wo,
                                                 const char* Are, const char* Aim, const char* Bre, const char* Bim,
                                                 const char* Cre, const char* Cim, const char* logdt, const char* dsk,
                                                 const char* gw, char* ws, char* out) {
  extern __shared__ __attribute__((aligned(16))) char smem[];
  LAS char* sm = (LAS char*)smem;
  if (BID < 256) attn_block(BID, x, wqkv, wphi, wo, ws, out, sm);
  else ssm_block(BID - 256, x, Are, Aim, Bre, Bim, Cre, Cim, logdt, dsk, gw, ws, sm);
}

NOPK_POP
extern "C" void kernel_launch(void* const* d_in, const int* in_sizes, int n_in, void* d_out, int out_size, void* d_ws,
                              size_t ws_size, hipStream_t stream) {
  (void)in_sizes; (void)n_in; (void)out_size; (void)ws_size;
  (void)hipFuncSetAttribute((const void*)k_all, hipFuncAttributeMaxDynamicSharedMemorySize, 81920);
  const char* const* in = (const char* const*)d_in;
  k_all<<<512, 256, 81920, stream>>>(in[0], in[1], in[2], in[3], in[5], in[6], in[7], in[8], in[9], in[10], in[11],
                                     in[12], in[13], (char*)d_ws, (char*)d_out);
}
```
